```python
import torch

def _softmax(x, axis=-1):
    return torch.softmax(x, dim=axis)
def _gelu(x):
    return torch.nn.functional.gelu(x, approximate="tanh")
def _silu(x):
    return torch.nn.functional.silu(x)
def _sigmoid(x):
    return torch.sigmoid(x)
def _layernorm(x, axis=-1, eps=1e-5):
    m = x.mean(dim=axis, keepdim=True)
    v = x.var(dim=axis, keepdim=True, unbiased=False)
    return (x - m) / torch.sqrt(v + eps)
def _scatter_add(tgt, idx, src, axis=0):
    return tgt.clone().index_add_(axis, idx, src)
def _segment_sum(src, seg, n):
    out = torch.zeros((n,) + src.shape[1:], dtype=src.dtype)
    return out.index_add_(0, seg, src)
def _pad(x, pw):
    flat = tuple(v for pair in reversed(pw) for v in pair)
    return torch.nn.functional.pad(x, flat)

kernel_name = 'interleave_layers__softmax_full_mha__glu_gated_swiglu__prompt'

def setup_inputs():

    _w = (lambda a: (lambda t: t.to(torch.bfloat16) if t.is_floating_point() else t)(torch.as_tensor(a)))
    def _A_setup():
        B, S, D = 1, 2048, 576
        return {
            "x": _w((lambda *s: torch.randn(*s, dtype=torch.bfloat16) * 0.05)(B, S, D)),
            "wqkv": _w((lambda *s: torch.randn(*s, dtype=torch.bfloat16) * 0.05)(D, 3 * D)),
            "wo": _w((lambda *s: torch.randn(*s, dtype=torch.bfloat16) * 0.05)(D, D)),
        }
    _A_in = _A_setup()

    def _B_setup():
        B, S, D, F = 1, 2048, 576, 4608
        return {
            "x": _w((lambda *s: torch.randn(*s, dtype=torch.bfloat16) * 0.05)(B, S, D)),
            "w1": _w((lambda *s: torch.randn(*s, dtype=torch.bfloat16) * 0.4)(D, F) * (2.0 / D) ** 0.5),
            "w3": _w((lambda *s: torch.randn(*s, dtype=torch.bfloat16) * 0.4)(D, F) * (2.0 / D) ** 0.5),
            "w2": _w((lambda *s: torch.randn(*s, dtype=torch.bfloat16) * 0.4)(F, D) * (2.0 / F) ** 0.5),
        }
    _B_in = _B_setup()

    return {
        "A_x": _A_in["x"],
        "A_wqkv": _A_in["wqkv"],
        "A_wo": _A_in["wo"],
        "B_x": _B_in["x"],
        "B_w1": _B_in["w1"],
        "B_w3": _B_in["w3"],
        "B_w2": _B_in["w2"],
    }

def reference(A_x, A_wqkv, A_wo, B_x, B_w1, B_w3, B_w2):
    def _A_ref(x, wqkv, wo):
        B, S, D = x.shape
        H = 32; Dh = D // H
        qkv = (lambda x, *d: x.permute(*d))((x @ wqkv).reshape(B, S, 3, H, Dh), 2, 0, 3, 1, 4)
        q, k, v = qkv[0], qkv[1], qkv[2]
        scale = Dh ** -0.5
        scores = torch.einsum("bhqd,bhkd->bhqk", q, k) * scale
        p = _softmax(scores, axis=-1)
        o = torch.einsum("bhqk,bhkd->bhqd", p, v)
        return (lambda x, *d: x.permute(*d))(o, 0, 2, 1, 3).reshape(B, S, D) @ wo

    def _B_ref(x, w1, w3, w2):
        gate = torch.einsum("bsd,df->bsf", x, w1)
        up = torch.einsum("bsd,df->bsf", x, w3)
        h = _silu(gate) * up
        out = torch.einsum("bsf,fd->bsd", h, w2)
        return out

    x = A_x
    x = x + _A_ref(_layernorm(x, axis=-1), A_wqkv, A_wo)
    x = x + _B_ref(_layernorm(x, axis=-1), B_w1, B_w3, B_w2)
    x = x + _A_ref(_layernorm(x, axis=-1), A_wqkv, A_wo)
    x = x + _B_ref(_layernorm(x, axis=-1), B_w1, B_w3, B_w2)
    return x
```

```cpp
#include <hip/hip_runtime.h>
#include <hip/hip_bf16.h>
#include <stdint.h>

typedef short bf16x8 __attribute__((ext_vector_type(8)));
typedef short s16x4 __attribute__((ext_vector_type(4)));
typedef float f32x16 __attribute__((ext_vector_type(16)));
typedef float f32x4 __attribute__((ext_vector_type(4)));
typedef unsigned u32x4 __attribute__((ext_vector_type(4)));
typedef unsigned u32x2 __attribute__((ext_vector_type(2)));
typedef __bf16 bf2_t __attribute__((ext_vector_type(2)));
typedef float f32x2 __attribute__((ext_vector_type(2)));
#define LAS __attribute__((address_space(3)))
#define DI __device__ __forceinline__

constexpr int S_ = 2048, D_ = 576, H_ = 32, DH_ = 18, F_ = 4608;
constexpr int KS16 = D_ / 16;
constexpr size_t SZ_WQ = 64u * 36 * 1024, SZ_WO = 8u * 18 * 4608, SZ_W13 = 288u * 36 * 1024, SZ_W2 = 8u * 144 * 4608;
constexpr size_t SZ_CSQ = 9u * 64 * 32 * 4, SZ_CS13 = 9u * 288 * 32 * 4, SZ_X = (size_t)S_ * D_ * 2;
constexpr int KV_HALF = 77824, KV_HEAD = 2 * KV_HALF, KV_K2 = 32768, KV_V = 36864;
constexpr size_t SZ_KV = (size_t)H_ * KV_HEAD, SZ_QT = (size_t)H_ * 65536, SZ_Q2 = (size_t)H_ * 8192, SZ_HT = (size_t)S_ * F_ * 2;
constexpr size_t OFF_WQ = 0, OFF_WO = OFF_WQ + SZ_WQ, OFF_W13 = OFF_WO + SZ_WO, OFF_W2 = OFF_W13 + SZ_W13,
                 OFF_CSQ = OFF_W2 + SZ_W2, OFF_CS13 = OFF_CSQ + SZ_CSQ, OFF_XA = OFF_CS13 + SZ_CS13, OFF_XB = OFF_XA + SZ_X,
                 OFF_KV = OFF_XB + SZ_X, OFF_QT = OFF_KV + SZ_KV, OFF_Q2 = OFF_QT + SZ_QT, OFF_OT = OFF_Q2 + SZ_Q2,
                 OFF_HT = OFF_OT + SZ_X, OFF_STA = OFF_HT + SZ_HT, OFF_STB = OFF_STA + S_ * 128, OFF_FL = OFF_STB + S_ * 64,
                 OFF_EX = OFF_FL + 2 * 64 * 128, WS_TOTAL = OFF_EX + 64u * 4 * 4 * 10240;

#define MFMA32(a, b, c) __builtin_amdgcn_mfma_f32_32x32x16_bf16(a, b, c, 0, 0, 0)
#define MFMA16(a, b, c) __builtin_amdgcn_mfma_f32_16x16x32_bf16(a, b, c, 0, 0, 0)
#define SBAR() __builtin_amdgcn_sched_barrier(0)
#define PIN(x) asm volatile("" : "+v"(x))

#define PROF_DECL
#define STAMP(i)
#define PROF_END(wsp, kid)
DI unsigned pk2(float lo, float hi) { f32x2 v = {lo, hi}; return __builtin_bit_cast(unsigned, __builtin_convertvector(v, bf2_t)); }
DI float bflo(unsigned u) { return __uint_as_float(u << 16); }
DI float bfhi(unsigned u) { return __uint_as_float(u & 0xffff0000u); }
DI float xhalf(float v) {
  auto rr = __builtin_amdgcn_permlane32_swap(__float_as_uint(v), __float_as_uint(v), false, false);
  return (threadIdx.x & 32) ? __uint_as_float(rr[0]) : __uint_as_float(rr[1]);
}
DI void set_m0(unsigned lds_base) { asm volatile("s_mov_b32 m0, %0" ::"s"(lds_base) : "memory"); }
template <int OFF> DI void glds(const char* base, unsigned voff) {
  asm volatile("global_load_lds_dwordx4 %0, %1 offset:%2" ::"v"(voff), "s"(base), "i"(OFF) : "memory");
}
template <int OFF> DI void glds16(const char* base, unsigned voff, unsigned lds_base) {
  asm volatile("s_mov_b32 m0, %2\n\ts_nop 0\n\tglobal_load_lds_dwordx4 %0, %1 offset:%3" ::"v"(voff), "s"(base), "s"(lds_base), "i"(OFF) : "memory");
}
template <int OFF> DI void gload16(bf16x8& d, const char* base, unsigned voff) {
  asm volatile("global_load_dwordx4 %0, %1, %2 offset:%3" : "=v"(d) : "v"(voff), "s"(base), "i"(OFF) : "memory");
}
DI void gload4(unsigned& d, const char* base, unsigned voff) {
  asm volatile("global_load_dword %0, %1, %2" : "=v"(d) : "v"(voff), "s"(base) : "memory");
}
#define WT 1
DI void st16(char* p, const u32x4& v) { if (WT) asm volatile("global_store_dwordx4 %0, %1, off sc1\n\ts_nop 1" ::"v"(p), "v"(v) : "memory"); else *(u32x4*)p = v; }
#define PWT 1
DI void PST(char* base, unsigned off, const u32x4& v) {
  if (PWT) asm volatile("global_store_dwordx4 %0, %1, %2 sc1\n\ts_nop 1" ::"v"(off), "v"(v), "s"(base) : "memory"); else *(u32x4*)(base + off) = v;
}
template <int N> DI void wait_vm() { asm volatile("s_waitcnt vmcnt(%0)" ::"i"(N) : "memory"); }
template <int N> DI void wait_vm_bar() { asm volatile("s_waitcnt vmcnt(%0)\n\ts_barrier" ::"i"(N) : "memory"); }
template <int N> DI void wait_vm_lgkm_bar() { asm volatile("s_waitcnt vmcnt(%0) lgkmcnt(0)\n\ts_barrier" ::"i"(N) : "memory"); }
template <int I> struct IC { static constexpr int v = I; };
template <int B, int E, class Fn> DI void sfor(Fn&& f) { if constexpr (B < E) { f(IC<B>{}); sfor<B + 1, E>(f); } }
DI bf16x8 ldsr(unsigned addr) { return *(const LAS bf16x8*)addr; }
#define HAUX 16
#define ROT 0
DI int rot9(int v) { return v >= 9 ? v - 9 : v; }
DI int swap23(int v) { return (v & ~12) | ((v & 4) << 1) | ((v & 8) >> 1); }

DI f32x2 stats8(const u32x4& v) {
  float s1 = 0.f, s2 = 0.f;
#pragma unroll
  for (int i = 0; i < 4; ++i) { const float lo = bflo(v[i]), hi = bfhi(v[i]); s1 += lo + hi; s2 = fmaf(lo, lo, fmaf(hi, hi, s2)); }
  return f32x2{s1, s2};
}
constexpr int NJW = 648 * 3 + 81;
struct WJob { int kind, kb, nb; };
DI WJob wjob(int job) {
  WJob j;
  if (job < 1296) { j.kind = job >= 648; const int q = job - j.kind * 648; j.kb = q / 72; j.nb = q % 72; }
  else if (job < 1944) { j.kind = 2; const int q = job - 1296; j.kb = q / 9; j.nb = q % 9; }
  else { j.kind = 3; const int q = job - 1944; j.kb = q / 9; j.nb = q % 9; }
  return j;
}
DI void wjob_load(const WJob& j, int lane, const char* src, unsigned (&r)[32]) {
  const int ld = j.kind < 2 ? F_ * 2 : j.kind == 4 ? 3 * D_ * 2 : D_ * 2;
  int r0 = j.kb * 64, rk = 8, ra = 16, rb = 32, j0 = 1, j1 = 2, j2 = 4;
  if (j.kind == 2) { ra = 1; j0 = 2; j1 = 4; j2 = 16; }
  if (j.kind == 3) {
    if (j.kb < 8) { r0 = j.kb * 72; rk = 1; ra = 18; rb = 36; j0 = 2; j1 = 4; j2 = 8; }
    else { r0 = 16; rk = 72; ra = 144; rb = 288; j1 = 18; j2 = 36; }
  }
  const unsigned voff = (lane >> 5) * rk * ld + (lane & 31) * 4;
  src += (size_t)r0 * ld + j.nb * 128;
#pragma unroll
  for (int i = 0; i < 32; ++i)
    gload4(r[i], src + (size_t)(((i >> 3) & 1) * ra + (i >> 4) * rb + (i & 1) * j0 + ((i >> 1) & 1) * j1 + ((i >> 2) & 1) * j2) * ld, voff);
}
DI void wjob_store(const WJob& j, int lane, const unsigned (&r)[32], char* ws) {
  const int kg = lane >> 5, cp = lane & 31, n = j.nb * 64 + cp * 2;
  unsigned off, es, p1 = 1024, p2 = 2048, cso = 0, ce = 0;
  if (j.kind < 2) {
    const int chunk = j.nb * 4 + (cp >> 4) * 2, rho = cp & 15;
    off = OFF_W13 + ((chunk * 72 + j.kb * 8 + kg) * 32 + j.kind * 16 + rho) * 16; es = 36864;
    cso = OFF_CS13 + ((j.kb * 288 + chunk) * 32 + ((rho >> 2) & 1) * 16 + j.kind * 8 + (rho >> 3) * 4 + (rho & 3)) * 4; ce = 128;
  } else if (j.kind == 2) {
    const int cq = n / 144, cc = n % 144;
    off = OFF_W2 + (cq * 144 + j.kb * 2) * 9216 + kg * 256; p1 = 512; p2 = 9216;
    if (cc < 128) { off += (((cc >> 5) - j.kb / 18 - 1) & 3) * 2048 + (cc & 31) * 8; es = 1024; }
    else { off += 8192 + (cc - 128) * 16; es = 16; }
  } else if (j.kind == 3) {
    const int cg = n / 72, cc = n % 72;
    off = OFF_WO + (cg * 18 + j.kb * 2) * 4608; p2 = 4608;
    if (cc < 64) { off += (cc >> 5) * 2048 + kg * 256 + (cc & 31) * 8; es = 1024; p1 = 512; }
    else { off += 4096 + kg * 128 + (cc - 64) * 16; es = 16; p1 = 256; }
  } else {
    const int part = n / D_, col = n % D_, h = col / DH_, d = col % DH_;
    int chunk, rho;
    if (d < 16) { chunk = part == 2 ? 2 * h + 1 : 2 * h; rho = (part == 0 ? 16 : 0) + (d >> 1); es = 128; ce = 16; }
    else { chunk = 2 * h + 1; rho = part == 0 ? 24 : 20 - 2 * part; es = 16; ce = 4; }
    off = OFF_WQ + ((chunk * 72 + j.kb * 8 + kg) * 32 + rho) * 16;
    cso = OFF_CSQ + ((j.kb * 64 + chunk) * 32 + ((rho >> 2) & 1) * 16 + (rho >> 3) * 4 + (rho & 3)) * 4;
  }
  float cs[2] = {0.f, 0.f};
#pragma unroll
  for (int pp = 0; pp < 4; ++pp)
#pragma unroll
    for (int e = 0; e < 2; ++e) {
      u32x4 v;
#pragma unroll
      for (int q = 0; q < 4; ++q) {
        const unsigned u = __builtin_amdgcn_perm(r[pp * 8 + 2 * q + 1], r[pp * 8 + 2 * q], e ? 0x07060302u : 0x05040100u);
        v[q] = u;
        cs[e] = __builtin_amdgcn_fdot2_f32_bf16(__builtin_bit_cast(bf2_t, u), __builtin_bit_cast(bf2_t, 0x3f803f80u), cs[e], false);
      }
      PST(ws, off + e * es + (pp & 1) * p1 + (pp >> 1) * p2, v);
    }
  if (j.kind < 2 || j.kind == 4) {
    cs[0] += xhalf(cs[0]); cs[1] += xhalf(cs[1]);
    *(float*)(ws + cso + kg * ce) = kg ? cs[1] : cs[0];
  }
}

constexpr int NJ_Q = 27 * 9, NJ_X = 256, NJ_Z = 32, NJ_ALL = NJ_Q + NJ_X + NJ_Z;
__global__ __launch_bounds__(64) void k_prep(const char* __restrict__ x, const char* __restrict__ wqkv, char* __restrict__ ws) {
  const int lane = threadIdx.x, job = blockIdx.x;
  if (job < NJ_Q) {
    const WJob j = {4, job / 27, job % 27};
    unsigned r[32];
    wjob_load(j, lane, wqkv, r);
    wait_vm<0>();
#pragma unroll
    for (int i = 0; i < 32; ++i) PIN(r[i]);
    wjob_store(j, lane, r, ws);
  } else if (job < NJ_Q + NJ_X) {
    const int tok = (job - NJ_Q) * 8 + (lane & 7), pg = lane >> 3;
    u32x4 v[9];
#pragma unroll
    for (int i = 0; i < 9; ++i) v[i] = *(const u32x4*)(x + (size_t)tok * (D_ * 2) + (i * 8 + pg) * 16);
    f32x2 a = {0.f, 0.f};
#pragma unroll
    for (int i = 0; i < 9; ++i) {
      *(u32x4*)(ws + OFF_XA + ((size_t)((tok >> 5) * 72 + i * 8 + pg) * 32 + (tok & 31)) * 16) = v[i];
      a += stats8(v[i]);
    }
    *(f32x4*)(ws + OFF_STA + (size_t)tok * 128 + pg * 16) = f32x4{a[0], a[1], 0.f, 0.f};
  } else {
    const int h = job - NJ_Q - NJ_X, c = 2 * h + 1;
    for (int i = lane; i < 72 * 10; i += 64) {
      const int p = i / 10, q = i % 10, rho = q < 4 ? 20 + q : 22 + q;
      *(u32x4*)(ws + OFF_WQ + ((size_t)(c * 72 + p) * 32 + rho) * 16) = u32x4{0, 0, 0, 0};
    }
    if (lane < 4) *(unsigned*)(ws + OFF_FL + (h * 4 + lane) * 128) = 0u;
  }
}

DI void stats_fin(const f32x4& a, const f32x4& b, float& mu, float& rstd, float& ve) {
  float s1 = (a[0] + a[2]) + (b[0] + b[2]), s2 = (a[1] + a[3]) + (b[1] + b[3]);
  s1 += xhalf(s1); s2 += xhalf(s2);
  mu = s1 * (1.f / D_);
  const float var = fmaxf(s2 * (1.f / D_) - mu * mu, 0.f);
  ve = var + 1e-5f;
  rstd = __builtin_amdgcn_rsqf(ve);
}

constexpr int QKV_LDS = 4 * 36 * 1024 + 512;
__global__ __launch_bounds__(256, 2) void k_qkv(const char* __restrict__ xt, char* __restrict__ ws) {
  extern __shared__ __attribute__((aligned(16))) char smem[];
  const int tid = threadIdx.x, lane = tid & 63, hh = lane >> 5, w = __builtin_amdgcn_readfirstlane(tid >> 6);
  const int xcd = blockIdx.x & 7, idx = blockIdx.x >> 3;
  const int mt = (xcd & 3) * 4 + (idx & 3), hp = (xcd >> 2) * 8 + (idx >> 2);
  const int tt = mt * 4 + w;
  PROF_DECL;
  const unsigned lds0 = (unsigned)(uintptr_t)smem;
  const char* wsrc = ws + OFF_WQ + (size_t)(hp * 4 + w) * 36 * 1024;
  const char* xsrc = xt + (size_t)tt * 36 * 1024;
  const unsigned l16 = lane * 16;
  float* csl = (float*)(smem + 4 * 36 * 1024);
  unsigned csr[9];
#pragma unroll
  for (int i = 0; i < 9; ++i) gload4(csr[i], ws + OFF_CSQ + (hp * 128 + i * 2048) * 4, (tid & 127) * 4);
  bf16x8 sta, stb, stc, std_;
  gload16<0>(sta, ws + OFF_STA + tt * 4096, (lane & 31) * 128 + hh * 64);
  gload16<16>(stb, ws + OFF_STA + tt * 4096, (lane & 31) * 128 + hh * 64);
  gload16<32>(stc, ws + OFF_STA + tt * 4096, (lane & 31) * 128 + hh * 64);
  gload16<48>(std_, ws + OFF_STA + tt * 4096, (lane & 31) * 128 + hh * 64);
  bf16x8 xf[36];
  constexpr int QD = 4;
  const int rot = ROT ? rot9((idx & 3) * 2 + (idx >> 2)) : 0;
  auto issue = [&](auto ic) {
    constexpr int s = decltype(ic)::v >> 1;
    if constexpr (s < 36) {
      const int o = rot9((s >> 2) + rot) * 4096;
      if constexpr (decltype(ic)::v & 1) glds<(s & 3) * 1024>(wsrc + o, l16);
      else {
        if constexpr ((s & 3) == 0) set_m0(lds0 + w * 36864 + (s >> 2) * 4096);
        gload16<(s & 3) * 1024>(xf[s], xsrc + o, l16);
      }
    }
  };
  sfor<0, 6 * QD>(issue);
  f32x16 acc[4] = {};
  unsigned lb[4];
#pragma unroll
  for (int c = 0; c < 4; ++c) { lb[c] = lds0 + l16 + c * 36864; PIN(lb[c]); }
  constexpr int PD = 6;
  bf16x8 ring[144];
  sfor<0, 144 + PD>([&](auto jc) {
    constexpr int r = decltype(jc)::v, m = r - PD;
    if constexpr (r < 144 && r % 12 == 0) {
      constexpr int g = r / 12;
      wait_vm_bar<6 * (g + QD <= 12 ? QD - 1 : 11 - g)>();
      if constexpr (g % 3 == 0) STAMP(1 + g / 3);
      if constexpr (g == 0) {
        float cst = 0.f;
#pragma unroll
        for (int i = 0; i < 9; ++i) { PIN(csr[i]); cst += __uint_as_float(csr[i]); }
        csl[tid & 127] = cst;
      }
#pragma unroll
      for (int s = 3 * g; s < 3 * g + 3; ++s) PIN(xf[s]);
    }
    if constexpr (r < 144 && r % 2 == 0) issue(IC<6 * QD + r / 2>{});
    if constexpr (r < 144) ring[r] = ldsr(lb[r & 3] + (r >> 2) * 1024);
    if constexpr (m >= 0) {
      acc[m & 3] = MFMA32(ring[m], xf[m >> 2], acc[m & 3]);
      PIN(acc[m & 3]);
    }
    SBAR();
  });
  STAMP(7);
  float mu, rstd, ve;
  PIN(sta); PIN(stb); PIN(stc); PIN(std_);
  stats_fin(__builtin_bit_cast(f32x4, sta) + __builtin_bit_cast(f32x4, stc), __builtin_bit_cast(f32x4, stb) + __builtin_bit_cast(f32x4, std_), mu, rstd, ve);
  const float nmr = -mu * rstd;
  constexpr float QS = 0.23570226039551584f * 1.4426950408889634f;
  const int tok = tt * 32 + (lane & 31);
#pragma unroll
  for (int hq = 0; hq < 2; ++hq) {
    const int head = hp * 2 + hq;
    float v[2][16];
#pragma unroll
    for (int c = 0; c < 2; ++c)
#pragma unroll
      for (int r4 = 0; r4 < 4; ++r4) {
        const f32x4 cs = *(const f32x4*)(csl + (hq * 2 + c) * 32 + hh * 16 + r4 * 4);
#pragma unroll
        for (int i = 0; i < 4; ++i) v[c][r4 * 4 + i] = acc[hq * 2 + c][r4 * 4 + i] * rstd + cs[i] * nmr;
      }
    char* kv = ws + OFF_KV + (size_t)head * KV_HEAD + (tok >> 10) * KV_HALF;
    st16(kv + (tt & 31) * 1024 + lane * 16, u32x4{pk2(v[0][0], v[0][1]), pk2(v[0][2], v[0][3]), pk2(v[0][4], v[0][5]), pk2(v[0][6], v[0][7])});
    st16(ws + OFF_QT + (size_t)head * 65536 + tt * 1024 + lane * 16,
         u32x4{pk2(v[0][8] * QS, v[0][9] * QS), pk2(v[0][10] * QS, v[0][11] * QS), pk2(v[0][12] * QS, v[0][13] * QS), pk2(v[0][14] * QS, v[0][15] * QS)});
    char* vr = kv + KV_V + (tok & 1023) * 40;
    *(u32x2*)(vr + hh * 16) = u32x2{pk2(v[1][0], v[1][1]), pk2(v[1][2], v[1][3])};
    *(u32x2*)(vr + hh * 16 + 8) = u32x2{pk2(v[1][4], v[1][5]), pk2(v[1][6], v[1][7])};
    if (hh == 0) {
      *(u32x2*)(vr + 32) = u32x2{pk2(v[1][8], v[1][9]), 0x00003f80u};
      *(unsigned*)(kv + KV_K2 + (tok & 1023) * 4) = pk2(v[1][10], v[1][11]);
      *(unsigned*)(ws + OFF_Q2 + (size_t)head * 8192 + tok * 4) = pk2(v[1][12] * QS, v[1][13] * QS);
    }
  }
  PROF_END(ws, 0);
}

constexpr int ATT_LDS = KV_HEAD + 2048;
#define MFMA8(a, b, c) __builtin_amdgcn_mfma_f32_32x32x8bf16_1k(a, b, c, 0, 0, 0)
DI s16x4 vtr(unsigned p) { return __builtin_amdgcn_ds_read_tr16_b64_v4i16((LAS s16x4*)p); }
DI bf16x8 vtr2(unsigned p) { return __builtin_shufflevector(vtr(p), vtr(p + 320), 0, 1, 2, 3, 4, 5, 6, 7); }
DI float max16(const f32x16& s) {
  float a = fmaxf(fmaxf(s[0], s[1]), s[2]), b = fmaxf(fmaxf(s[3], s[4]), s[5]);
  a = fmaxf(fmaxf(a, s[6]), s[7]); b = fmaxf(fmaxf(b, s[8]), s[9]);
  a = fmaxf(fmaxf(a, s[10]), s[11]); b = fmaxf(fmaxf(b, s[12]), s[13]);
  a = fmaxf(fmaxf(a, s[14]), s[15]);
  return fmaxf(a, b);
}
DI s16x4 make_qb(unsigned q2, float m, int hh) {
  const float nm = -m;
  const float h0 = bfhi(__float_as_uint(nm)), r0 = nm - h0, h1 = bfhi(__float_as_uint(r0)), r1 = r0 - h1;
  u32x2 u = {q2, (__float_as_uint(h0) >> 16) | __float_as_uint(h1)};
  if (hh) u = u32x2{0u, __float_as_uint(r1) >> 16};
  return __builtin_bit_cast(s16x4, u);
}
#define EX(i) e[i] = __builtin_amdgcn_exp2f(sc[i]); PIN(e[i])

template <bool FIRST>
__global__ __launch_bounds__(512, 2) void k_attn(char* __restrict__ ws, const char* __restrict__ w1, const char* __restrict__ w3,
                                                 const char* __restrict__ w2, const char* __restrict__ wo) {
  extern __shared__ __attribute__((aligned(16))) char smem[];
  const int tid = threadIdx.x, lane = tid & 63, hh = lane >> 5, w = __builtin_amdgcn_readfirstlane(tid >> 6);
  const int xcd = blockIdx.x & 7, idx = blockIdx.x >> 3;
  const int head = xcd * 4 + (idx & 3), qb = idx >> 2;
  const int qg = w & 3, kh = w >> 2;
  PROF_DECL;
  const unsigned lds0 = (unsigned)(uintptr_t)smem + kh * KV_HALF, l16 = lane * 16;
  const char* kvsrc = ws + OFF_KV + (size_t)head * KV_HEAD + kh * KV_HALF;
  bf16x8 qa[2]; unsigned q2[2];
#pragma unroll
  for (int q = 0; q < 2; ++q) {
    const int qt = qb * 8 + qg * 2 + q;
    gload16<0>(qa[q], ws + OFF_QT + (size_t)head * 65536 + qt * 1024, l16);
    gload4(q2[q], ws + OFF_Q2 + (size_t)head * 8192 + qt * 128, (lane & 31) * 4);
  }
#pragma unroll
  for (int i = 0; i < 5; ++i) {
    int p = qg * 5 + i; p = p > 18 ? 18 : p;
    const int off = (p < 8 ? p : p == 8 ? 32 : 27 + p) * 1024;
    glds16<0>(kvsrc + off, l16, lds0 + off);
  }
  auto late = [&](int i) {
    int p = i * 4 + qg; p = p > 56 ? 56 : p;
    const int off = (p < 24 ? 8 + p : p < 27 ? 9 + p : 19 + p) * 1024;
    glds16<0>(kvsrc + off, l16, lds0 + off);
  };
  __builtin_amdgcn_s_setprio(1);
  const int i16 = lane & 15;
  const unsigned kp0 = lds0 + l16, k2p0 = lds0 + KV_K2 + (lane & 31) * 4;
  const unsigned vp0 = lds0 + KV_V + (4 * hh + (i16 >> 2)) * 40 + ((lane & 16) ? 32 : (i16 & 3) * 8);
  const f32x16 zero = {};
  f32x16 o[2];
  float m[2];
  u32x2 kb = {0u, 0x3f803f80u};
  PIN(kb);
  STAMP(1);
  wait_vm_bar<0>();
  STAMP(2);
  PIN(qa[0]); PIN(qa[1]); PIN(q2[0]); PIN(q2[1]);
  {
    const bf16x8 ka = ldsr(kp0);
    kb[0] = *(const LAS unsigned*)k2p0;
#pragma unroll
    for (int q = 0; q < 2; ++q) {
      f32x16 s = MFMA32(ka, qa[q], zero);
      s = MFMA8(__builtin_bit_cast(s16x4, kb), make_qb(q2[q], 0.f, hh), s);
      float mx = max16(s);
      m[q] = fmaxf(mx, xhalf(mx));
    }
  }
  for (int pass = 0; pass < 2; ++pass) {
    const s16x4 qb2[2] = {make_qb(q2[0], m[0], hh), make_qb(q2[1], m[1], hh)};
    o[0] = zero; o[1] = zero;
    unsigned kp = kp0, k2p = k2p0, vp = vp0;
    PIN(kp); PIN(k2p); PIN(vp);
    bf16x8 ka = ldsr(kp);
    kb[0] = *(const LAS unsigned*)k2p;
    bf16x8 v0 = vtr2(vp), v1 = vtr2(vp + 640);
    f32x16 sa = MFMA32(ka, qa[0], zero), sb;
    sa = MFMA8(__builtin_bit_cast(s16x4, kb), qb2[0], sa);
    u32x4 p1 = {0u, 0u, 0u, 0u};
    float ep[4] = {0.f, 0.f, 0.f, 0.f};
    auto half = [&](auto qc, auto tc, const f32x16& sc, f32x16& sn) {
      constexpr int Q = decltype(qc)::v, T = decltype(tc)::v;
      float e[16]; u32x4 p0;
      sn = MFMA32(ka, qa[Q ^ 1], zero);
      SBAR();
      if constexpr (Q == 0) ka = ldsr(kp + (T + 1) * 1024);
      p1[2] = pk2(ep[0], ep[1]); p1[3] = pk2(ep[2], ep[3]); PIN(p1); EX(0); EX(1); EX(2); EX(3);
      SBAR();
      o[Q ^ 1] = MFMA32(v1, __builtin_bit_cast(bf16x8, p1), o[Q ^ 1]);
      SBAR();
      if constexpr (Q == 0) v1 = vtr2(vp + T * 1280 + 640);
      p0[0] = pk2(e[0], e[1]); p0[1] = pk2(e[2], e[3]); SBAR(); EX(4); EX(5); EX(6); EX(7);
      SBAR();
      sn = MFMA8(__builtin_bit_cast(s16x4, kb), qb2[Q ^ 1], sn);
      SBAR();
      if constexpr (Q == 0) kb[0] = *(const LAS unsigned*)(k2p + (T + 1) * 128);
      p0[2] = pk2(e[4], e[5]); p0[3] = pk2(e[6], e[7]); PIN(p0); EX(8); EX(9); EX(10); EX(11);
      SBAR();
      o[Q] = MFMA32(v0, __builtin_bit_cast(bf16x8, p0), o[Q]);
      SBAR();
      if constexpr (Q == 1) v0 = vtr2(vp + (T + 1) * 1280);
      p1[0] = pk2(e[8], e[9]); p1[1] = pk2(e[10], e[11]); SBAR(); EX(12); EX(13); EX(14); EX(15);
      ep[0] = e[12]; ep[1] = e[13]; ep[2] = e[14]; ep[3] = e[15];
      SBAR();
    };
    auto iter = [&](auto mc) {
      constexpr int MODE = decltype(mc)::v;
      sfor<0, 4>([&](auto tc) {
        constexpr int T = decltype(tc)::v;
        if constexpr (T == 0) asm volatile("s_cmp_eq_u32 %0, 0\n\ts_cbranch_scc1 1f\n\ts_setprio 2\n1:" ::"s"(kh) : "scc");
        if constexpr (T == 3) asm volatile("s_cmp_eq_u32 %0, 0\n\ts_cbranch_scc1 1f\n\ts_setprio 0\n1:" ::"s"(kh) : "scc");
        if constexpr (MODE == 1) { late(4 * T); late(4 * T + 1); }
        if constexpr (MODE == 2 && T == 3) { STAMP(3); wait_vm_bar<0>(); STAMP(4); }
        half(IC<0>{}, tc, sa, sb);
        if constexpr (MODE == 1) { late(4 * T + 2); if (T < 3) late(4 * T + 3); }
        half(IC<1>{}, tc, sb, sa);
      });
      kp += 4096; k2p += 512; vp += 5120;
    };
    const int job = blockIdx.x * 8 + w;
    unsigned wr[32];
    if (pass == 0) {
      iter(IC<1>{}); iter(IC<2>{});
      if (FIRST) { const WJob j = wjob(job < NJW ? job : 0); wjob_load(j, lane, j.kind == 0 ? w1 : j.kind == 1 ? w3 : j.kind == 2 ? w2 : wo, wr); }
    }
    for (int it = pass ? 0 : 2; it < 8; ++it) {
      if (FIRST && it == 4 + kh && pass == 0) {
        wait_vm<0>();
#pragma unroll
        for (int i = 0; i < 32; ++i) PIN(wr[i]);
        if (job < NJW) wjob_store(wjob(job), lane, wr, ws);
      }
      iter(IC<0>{});
    }
    p1[2] = pk2(ep[0], ep[1]); p1[3] = pk2(ep[2], ep[3]);
    o[1] = MFMA32(v1, __builtin_bit_cast(bf16x8, p1), o[1]);
    bool ok = true;
#pragma unroll
    for (int q = 0; q < 2; ++q) {
      float a = o[q][10];
#pragma unroll
      for (int r = 0; r < 10; ++r) a = fmaxf(a, fabsf(o[q][r]) * 0x1p-40f);
      ok = ok && (a < 0x1p60f);
#pragma unroll
      for (int r = 0; r < 11; ++r) ok = ok && (o[q][r] == o[q][r]);
    }
    if (pass == 1 || __all(ok)) break;
    const s16x4 qz0 = make_qb(q2[0], 0.f, hh), qz1 = make_qb(q2[1], 0.f, hh);
    float mx0 = m[0], mx1 = m[1];
    for (int t = 0; t < 32; ++t) {
      const bf16x8 kk = ldsr(kp0 + t * 1024);
      kb[0] = *(const LAS unsigned*)(k2p0 + t * 128);
      f32x16 s = MFMA32(kk, qa[0], zero); s = MFMA8(__builtin_bit_cast(s16x4, kb), qz0, s); mx0 = fmaxf(mx0, max16(s));
      s = MFMA32(kk, qa[1], zero); s = MFMA8(__builtin_bit_cast(s16x4, kb), qz1, s); mx1 = fmaxf(mx1, max16(s));
    }
    m[0] = fmaxf(mx0, xhalf(mx0)); m[1] = fmaxf(mx1, xhalf(mx1));
  }
  STAMP(5);
  __syncthreads();
  float* mg = (float*)smem + (qg * 24) * 64 + lane;
  if (kh == 1) {
#pragma unroll
    for (int q = 0; q < 2; ++q) {
#pragma unroll
      for (int r = 0; r < 11; ++r) mg[(q * 12 + r) * 64] = o[q][r];
      mg[(q * 12 + 11) * 64] = m[q];
    }
  }
  __syncthreads();
  if (kh == 0) {
#pragma unroll
    for (int q = 0; q < 2; ++q) {
      const float m1 = mg[(q * 12 + 11) * 64], mm = fmaxf(m[q], m1);
      const float a0 = __builtin_amdgcn_exp2f(m[q] - mm), a1 = __builtin_amdgcn_exp2f(m1 - mm);
      float r_[11];
#pragma unroll
      for (int r = 0; r < 11; ++r) r_[r] = o[q][r] * a0 + mg[(q * 12 + r) * 64] * a1;
      const float inv = 1.f / r_[10];
      const int tok = (qb * 8 + qg * 2 + q) * 32 + (lane & 31);
      char* ob = ws + OFF_OT + (size_t)(tok >> 4) * 18 * 1024 + (tok & 15) * 16;
      st16(ob + (2 * head + hh) * 256, u32x4{pk2(r_[0] * inv, r_[1] * inv), pk2(r_[2] * inv, r_[3] * inv), pk2(r_[4] * inv, r_[5] * inv), pk2(r_[6] * inv, r_[7] * inv)});
      if (hh == 0) *(unsigned*)(ob + (64 + (head >> 2)) * 256 + (head & 3) * 4) = pk2(r_[8] * inv, r_[9] * inv);
    }
  }
  PROF_END(ws, 1);
}

constexpr int UP_SLOT = 36 * 1024, UP_LDS = 4 * UP_SLOT + 9 * 128;
constexpr int UP_D = 4;
constexpr int up_x0(int j) { return j >= 14 ? 36 : (8 * j + 2) / 3; }
constexpr int up_n(int j) { return j < 14 ? 1 + up_x0(j + 1) - up_x0(j) : j < 16 ? 2 : j == 16 ? 1 : 0; }
constexpr int up_after(int j) { int n = 0; for (int i = j + 1; i < j + UP_D; ++i) n += up_n(i); return n; }
__global__ __launch_bounds__(512, 2) void k_up(const char* __restrict__ xt, char* __restrict__ ws) {
  extern __shared__ __attribute__((aligned(16))) char smem[];
  const int tid = threadIdx.x, lane = tid & 63, hh = lane >> 5, w = __builtin_amdgcn_readfirstlane(tid >> 6);
  const int xcd = blockIdx.x & 7, idx = blockIdx.x >> 3;
  const int mt = (xcd & 1) * 4 + (idx & 3), nt = (xcd >> 1) * 8 + (idx >> 2);
  const int tt = mt * 8 + w;
  PROF_DECL;
  const unsigned lds0 = (unsigned)(uintptr_t)smem;
  const char* wsrc = ws + OFF_W13 + (size_t)nt * 9 * UP_SLOT;
  const char* xsrc = xt + (size_t)tt * 36 * 1024;
  const unsigned l16 = lane * 16;
  float* csl = (float*)(smem + 4 * UP_SLOT);
  unsigned csr[9];
  const int csi = tid < 288 ? tid : 287;
#pragma unroll
  for (int i = 0; i < 9; ++i) gload4(csr[i], ws + OFF_CS13 + (nt * 288 + i * 9216) * 4, csi * 4);
  bf16x8 sta, stb;
  gload16<0>(sta, ws + OFF_STB + tt * 2048, (lane & 31) * 64 + hh * 32);
  gload16<16>(stb, ws + OFF_STB + tt * 2048, (lane & 31) * 64 + hh * 32);
  const int rot = ROT ? rot9((idx & 3) * 2 + (idx >> 2)) : 0;
  const int sw4 = rot9(w + rot) * 4096, sw1 = rot9(8 + rot) * 4096 + (w & 3) * 1024;
  auto stage = [&](auto cc, auto ic) {
    constexpr int c = decltype(cc)::v, i = decltype(ic)::v;
    if constexpr (i == 0) glds16<0>(wsrc + c * UP_SLOT + sw4, l16, lds0 + (c & 3) * UP_SLOT + w * 4096);
    else if constexpr (i < 4) glds<i * 1024>(wsrc + c * UP_SLOT + sw4, l16);
    else glds16<0>(wsrc + c * UP_SLOT + sw1, l16, lds0 + (c & 3) * UP_SLOT + 32768 + (w & 3) * 1024);
  };
  bf16x8 xf[36];
  auto item = [&](auto jc, auto ic) {
    constexpr int j = decltype(jc)::v, i = decltype(ic)::v;
    if constexpr (i < up_n(j)) {
      if constexpr (j >= 14) stage(IC<3>{}, IC<(j - 14) * 2 + i>{});
      else if constexpr (i == 0) {
        int p = 8 * j + w;
        if (j == 13) p = 104 + (w & 3);
        const int s = (p * 43) >> 7, off = (p - 3 * s) * UP_SLOT + (s & 3) * 1024;
        glds16<0>(wsrc + off + rot9((s >> 2) + rot) * 4096, l16, lds0 + off + (s >> 2) * 4096);
      } else {
        constexpr int s = up_x0(j) + i - 1;
        gload16<(s & 3) * 1024>(xf[s], xsrc + rot9((s >> 2) + rot) * 4096, l16);
      }
    }
  };
  sfor<0, UP_D>([&](auto jc) { sfor<0, 4>([&](auto ic) { item(jc, ic); }); });
  unsigned lb[4];
#pragma unroll
  for (int c = 0; c < 4; ++c) { lb[c] = lds0 + l16 + c * UP_SLOT; PIN(lb[c]); }
  unsigned csb = lds0 + 4 * UP_SLOT + hh * 64; PIN(csb);
  float nmu, ke, ve;
  f32x16 acc[9];
  f32x4 cs[37];
  u32x4 hv;
  const __amdgpu_buffer_rsrc_t rh =
      __builtin_amdgcn_make_buffer_rsrc((void*)(ws + OFF_HT + (size_t)(tt * 2) * 144 * 1024 + (size_t)nt * 9 * 512), 0, 2 * 144 * 1024, 0x00020000);
  const unsigned hoff = ((lane >> 4) & 1) * 144 * 1024 + hh * 256 + (lane & 15) * 16;
  auto ldcs = [&](int e) { cs[e] = *(const LAS f32x4*)(csb + (e >> 2) * 128 + (e & 3) * 16); };
  float g0, g1, u0, u1, t0, t1, h0, h1;
  auto epi = [&](auto ec, auto pc) {
    constexpr int e = decltype(ec)::v, c = e >> 2, i = (e & 3) * 2, p = decltype(pc)::v;
    if constexpr (p == 0) {
      const float a0 = acc[c][i], a1 = acc[c][i + 1], a2 = acc[c][8 + i], c0 = cs[e][0], c1 = cs[e][1], c2 = cs[e][2];
      asm volatile("v_fma_f32 %0, %3, %4, %5\n\tv_fma_f32 %1, %3, %6, %7\n\tv_fma_f32 %2, %3, %8, %9"
                   : "=&v"(g0), "=&v"(g1), "=v"(u0) : "v"(nmu), "v"(c0), "v"(a0), "v"(c1), "v"(a1), "v"(c2), "v"(a2));
    } else if constexpr (p == 1) {
      const float a3 = acc[c][9 + i], c3 = cs[e][3];
      asm volatile("v_mul_f32 %0, %3, %4\n\tv_mul_f32 %1, %3, %5\n\tv_fma_f32 %2, %6, %7, %8"
                   : "=&v"(t0), "=&v"(t1), "=v"(u1) : "v"(ke), "v"(g0), "v"(g1), "v"(nmu), "v"(c3), "v"(a3));
    } else if constexpr (p == 2) {
      asm volatile("v_exp_f32 %0, %0\n\tv_exp_f32 %1, %1\n\tv_mul_f32 %2, %2, %3" : "+v"(t0), "+v"(t1), "+v"(g0) : "v"(u0));
      if constexpr (e + 1 < 36) ldcs(e + 1);
    } else if constexpr (p == 3) {
      asm volatile("v_fma_f32 %0, %0, %3, %3\n\tv_fma_f32 %1, %1, %3, %3\n\tv_mul_f32 %2, %2, %4" : "+v"(t0), "+v"(t1), "+v"(g1) : "v"(ve), "v"(u1));
    } else if constexpr (p == 4) {
      asm volatile("v_rcp_f32 %0, %0\n\tv_rcp_f32 %1, %1" : "+v"(t0), "+v"(t1));
    } else if constexpr (p == 5) {
      asm volatile("v_mul_f32 %0, %2, %3\n\tv_mul_f32 %1, %4, %5" : "=&v"(h0), "=v"(h1) : "v"(g0), "v"(t0), "v"(g1), "v"(t1));
    } else {
      unsigned d;
      asm volatile("v_cvt_pk_bf16_f32 %0, %1, %2" : "=v"(d) : "v"(h0), "v"(h1));
      hv[e & 3] = d;
      if constexpr ((e & 3) == 3) __builtin_amdgcn_raw_buffer_store_b128(hv, rh, hoff + c * 512, 0, HAUX);
    }
  };
  constexpr int PD = 4;
  bf16x8 ring[324];
  sfor<0, 324 + PD>([&](auto jc) {
    constexpr int r = decltype(jc)::v, m = r - PD;
    if constexpr (r < 108) {
      constexpr int j = r >> 3;
      if constexpr ((r & 7) == 0) {
        wait_vm_bar<up_after(j)>();
#pragma unroll
        for (int s = up_x0(j); s < up_x0(j + 1); ++s) PIN(xf[s]);
        if constexpr (j == 0) {
          STAMP(1);
          float cst = 0.f;
#pragma unroll
          for (int i = 0; i < 9; ++i) { PIN(csr[i]); cst += __uint_as_float(csr[i]); }
          csl[(csi & ~15) + (csi & 6) * 2 + ((csi >> 3) & 1) * 2 + (csi & 1)] = cst;
          PIN(sta); PIN(stb);
          float mu, rstd; stats_fin(__builtin_bit_cast(f32x4, sta), __builtin_bit_cast(f32x4, stb), mu, rstd, ve);
          nmu = -mu; ke = rstd * -1.4426950408889634f;
          PIN(nmu); PIN(ke); PIN(ve);
        }
      }
      if constexpr ((r & 7) < 4) item(IC<j + UP_D>{}, IC<r & 7>{});
      ring[r] = ldsr(lb[r % 3] + (r / 3) * 1024);
    } else if constexpr (r < 324) {
      constexpr int c = 3 + (r - 108) / 36, s = (r - 108) % 36;
      if constexpr (s == 0) {
        if constexpr (c == 3) wait_vm_lgkm_bar<0>();
        else if constexpr (c < 8) wait_vm_bar<5>();
        else wait_vm_bar<0>();
        STAMP(c - 1);
      }
      if constexpr (c == 3 && s % 3 == 2 && s < 30) stage(IC<4 + (s / 3) / 5>{}, IC<(s / 3) % 5>{});
      if constexpr (c > 3 && c < 7 && s % 6 == 2 && s < 30) stage(IC<c + 2>{}, IC<s / 6>{});
      ring[r] = ldsr(lb[c & 3] + s * 1024);
    }
    if constexpr (m >= 0) {
      constexpr int c = m < 108 ? m % 3 : 3 + (m - 108) / 36, s = m < 108 ? m / 3 : (m - 108) % 36;
      if constexpr (s == 0) acc[c] = MFMA32(ring[m], xf[s], (f32x16){}); else acc[c] = MFMA32(ring[m], xf[s], acc[c]);
      PIN(acc[c]);
      if constexpr (m < 108) {
        if constexpr (m == 104) ldcs(0);
      } else {
        SBAR();
        constexpr int q = c < 7 ? m - 108 : s >= 4 && (s - 4) % 8 < 6 ? (c - 1) * 24 + (s - 4) / 8 * 6 + (s - 4) % 8 : -1;
        if constexpr (q >= 0) epi(IC<q / 6>{}, IC<q % 6>{});
        if constexpr (q >= 6 && q % 6 == 4) epi(IC<q / 6 - 1>{}, IC<6>{});
      }
    }
    SBAR();
  });
  STAMP(8);
  epi(IC<31>{}, IC<6>{});
  {
    ldcs(33); ldcs(34); ldcs(35);
    float h[8];
#pragma unroll
    for (int i = 0; i < 8; ++i) {
      const float g = acc[8][i] + cs[32 + (i >> 1)][i & 1] * nmu, u = acc[8][8 + i] + cs[32 + (i >> 1)][2 + (i & 1)] * nmu;
      h[i] = (g * u) * __builtin_amdgcn_rcpf(fmaf(__builtin_amdgcn_exp2f(g * ke), ve, ve));
    }
    __builtin_amdgcn_raw_buffer_store_b128(u32x4{pk2(h[0], h[1]), pk2(h[2], h[3]), pk2(h[4], h[5]), pk2(h[6], h[7])}, rh, hoff, 8 * 512, HAUX);
  }
  PROF_END(ws, 3);
}

template <int KS, int NW, int DEPTH, bool FINAL>
__global__ __launch_bounds__(NW * 64, 2) void k_out(const char* __restrict__ at, const char* __restrict__ wt,
                                                    const char* __restrict__ res, char* __restrict__ out, char* __restrict__ stats) {
  extern __shared__ __attribute__((aligned(16))) char smem[];
  constexpr int PER = KS / NW;
  static_assert(PER % DEPTH == 0, "");
  const int tid = threadIdx.x, lane = tid & 63, w = __builtin_amdgcn_readfirstlane(tid >> 6);
  const int xcd = blockIdx.x & 7, idx = blockIdx.x >> 3;
  const int mt = (xcd & 3) * 8 + (idx & 7), cg = (xcd >> 2) * 4 + (idx >> 3);
  const __amdgpu_buffer_rsrc_t ra = __builtin_amdgcn_make_buffer_rsrc((void*)(at + (size_t)(mt * 4) * KS * 1024), 0, 4 * KS * 1024, 0x00020000);
  const __amdgpu_buffer_rsrc_t rw = __builtin_amdgcn_make_buffer_rsrc((void*)(wt + (size_t)cg * KS * 4608), 0, KS * 4608, 0x00020000);
  const int l16 = lane * 16, l4 = 4096 + ((lane >> 4) * 8 + (lane & 7)) * 16;
  PROF_DECL;
  f32x4 acc[5][4] = {};
  bf16x8 af[DEPTH][4], wf[DEPTH][5];
  auto load = [&](int b, int i) {
    const int s = i * NW + w;
#pragma unroll
    for (int t = 0; t < 4; ++t) af[b][t] = __builtin_bit_cast(bf16x8, __builtin_amdgcn_raw_buffer_load_b128(ra, l16, (t * KS + s) * 1024, 0));
#pragma unroll
    for (int c = 0; c < 4; ++c) wf[b][c] = __builtin_bit_cast(bf16x8, __builtin_amdgcn_raw_buffer_load_b128(rw, l16 + c * 1024, s * 4608, 0));
    wf[b][4] = __builtin_bit_cast(bf16x8, __builtin_amdgcn_raw_buffer_load_b128(rw, l4, s * 4608, 0));
  };
  auto mma = [&](int b) {
#pragma unroll
    for (int c = 0; c < 5; ++c)
#pragma unroll
      for (int t = 0; t < 4; ++t) acc[c][t] = MFMA16(wf[b][c], af[b][t], acc[c][t]);
  };
#pragma unroll
  for (int b = 0; b < DEPTH; ++b) load(b, b);
  SBAR();
  for (int i0 = DEPTH; i0 < PER; i0 += DEPTH) {
#pragma unroll
    for (int b = 0; b < DEPTH; ++b) { mma(b); SBAR(); load(b, i0 + b); SBAR(); }
  }
#pragma unroll
  for (int b = 0; b < DEPTH; ++b) { mma(b); SBAR(); }
  STAMP(2);
  float* red = (float*)smem;
  f32x2* stl = (f32x2*)(smem + NW * 64 * 76 * 4);
  {
    float* p = red + (w * 64 + (lane & 15)) * 76 + (lane >> 4) * 4;
#pragma unroll
    for (int t = 0; t < 4; ++t)
#pragma unroll
      for (int c = 0; c < 5; ++c)
        if (c < 4 || lane < 32) *(f32x4*)(p + t * 16 * 76 + c * 16) = acc[c][t];
  }
  __syncthreads();
  STAMP(3);
  for (int i = tid; i < 576; i += NW * 64) {
    const int t = FINAL ? i / 9 : i & 63, pc = FINAL ? i % 9 : i >> 6;
    const int tok = mt * 64 + t, P = cg * 9 + pc;
    const size_t toff = ((size_t)((tok >> 5) * 72 + P) * 32 + (tok & 31)) * 16;
    const u32x4 rv = *(const u32x4*)(res + toff);
    f32x4 a = {}, b = {};
    const int o0 = pc < 8 ? (pc >> 2) * 32 + (pc & 3) * 4 : 64, o1 = pc < 8 ? 16 : 4;
#pragma unroll
    for (int ww = 0; ww < NW; ++ww) {
      const float* p = red + (ww * 64 + t) * 76 + o0;
      a += *(const f32x4*)p; b += *(const f32x4*)(p + o1);
    }
    if (pc == 8) { const f32x4 a2 = {a[0], a[2], b[0], b[2]}, b2 = {a[1], a[3], b[1], b[3]}; a = a2; b = b2; }
    const u32x4 ov = {pk2(a[0] + bflo(rv[0]), b[0] + bfhi(rv[0])), pk2(a[1] + bflo(rv[1]), b[1] + bfhi(rv[1])),
                      pk2(a[2] + bflo(rv[2]), b[2] + bfhi(rv[2])), pk2(a[3] + bflo(rv[3]), b[3] + bfhi(rv[3]))};
    if (FINAL) *(u32x4*)(out + ((size_t)tok * D_ + P * 8) * 2) = ov;
    else { st16(out + toff, ov); stl[i] = stats8(ov); }
  }
  if (!FINAL) {
    __syncthreads();
    if (tid < 64) {
      f32x2 a = stl[tid];
#pragma unroll
      for (int i = 1; i < 9; ++i) a += stl[i * 64 + tid];
      *(f32x2*)(stats + (size_t)(mt * 64 + tid) * 64 + cg * 8) = a;
    }
  }
  PROF_END(wt - (KS == 18 ? OFF_WO : OFF_W2), KS == 18 ? 2 : 4);
}

constexpr int DN_LDS = 128 * 1024, DN_D = 4;
constexpr int dn_s(int j) { return (8 * j + 8) / 9; }
constexpr int dn_n(int j) { return j > 20 ? 0 : 2 + 2 * ((j < 20 ? dn_s(j + 1) : 18) - dn_s(j)); }
constexpr int dn_after(int j) { int n = 0; for (int i = j + 1; i < j + DN_D; ++i) n += dn_n(i); return n; }
template <bool FINAL>
__global__ __launch_bounds__(512, 2) void k_down(char* __restrict__ ws, char* __restrict__ out) {
  extern __shared__ __attribute__((aligned(16))) char smem[];
  const int tid = threadIdx.x, lane = tid & 63, w = __builtin_amdgcn_readfirstlane(tid >> 6);
  const int xcd = blockIdx.x & 7, idx = blockIdx.x >> 3;
  const int kq = xcd >> 1, mt = (xcd & 1) * 8 + (idx & 7), cq = idx >> 3, tg = w & 3, kh = w >> 2;
  PROF_DECL;
  const unsigned lds0 = (unsigned)(uintptr_t)smem, l16 = lane * 16;
  const char* wsrc = ws + OFF_W2 + (size_t)(cq * 144 + kq * 36 + kh * 18) * 9216;
  const char* hsrc = ws + OFF_HT + (size_t)((mt * 8 + tg * 2) * 144 + kq * 36 + kh * 18) * 1024;
  const int tok = (mt * 8 + tg * 2 + kh) * 16 + (lane & 15), g = lane >> 4;
  const unsigned xoff = (tok >> 5) * 72 * 512 + (tok & 31) * 16;
  bf16x8 res; unsigned res8a, res8b;
  gload16<0>(res, ws + OFF_XB + (cq * 18 + kq * 4) * 512, xoff + g * 512);
  gload4(res8a, ws + OFF_XB + (cq * 18 + 16) * 512, xoff + (g >> 1) * 512 + (g & 1) * 8);
  gload4(res8b, ws + OFF_XB + (cq * 18 + 16) * 512 + 4, xoff + (g >> 1) * 512 + (g & 1) * 8);
  bf16x8 hf[18][2];
  auto item = [&](auto jc, auto ic) {
    constexpr int j = decltype(jc)::v, i = decltype(ic)::v;
    if constexpr (i < dn_n(j)) {
      const int p = j < 20 ? 8 * j + 2 * tg : 160;
      if constexpr (i == 0) glds16<0>(wsrc + p * 1024, l16, lds0 + kh * 65536 + (p & 63) * 1024);
      else if constexpr (i == 1) glds<1024>(wsrc + p * 1024, l16);
      else { constexpr int s = dn_s(j); gload16<(s & 3) * 1024>(hf[s][i - 2], hsrc + (i - 2) * 144 * 1024 + (s >> 2) * 4096, l16); }
    }
  };
  sfor<0, DN_D>([&](auto jc) { sfor<0, 4>([&](auto ic) { item(jc, ic); }); });
  unsigned lb = lds0 + kh * 65536 + l16; PIN(lb);
  f32x4 acc[9][2];
  constexpr int PD = 4;
  bf16x8 ring[162];
  sfor<0, 162 + PD>([&](auto jc) {
    constexpr int r = decltype(jc)::v, m = r - PD;
    if constexpr (r < 162) {
      constexpr int j = r >> 3;
      if constexpr ((r & 7) == 0) {
        wait_vm_bar<dn_after(j)>();
        if constexpr (dn_n(j) > 2) { PIN(hf[dn_s(j)][0]); PIN(hf[dn_s(j)][1]); }
        if constexpr (j == 0) STAMP(1);
        if constexpr (j == 10) STAMP(2);
      }
      if constexpr (r & 1) item(IC<j + DN_D>{}, IC<(r & 7) / 2>{});
      ring[r] = ldsr(lb + (r & 63) * 1024);
    }
    if constexpr (m >= 0) {
      constexpr int s = m / 9, c = m % 9;
#pragma unroll
      for (int t = 0; t < 2; ++t) {
        if constexpr (s == 0) acc[c][t] = MFMA16(ring[m], hf[s][t], (f32x4){}); else acc[c][t] = MFMA16(ring[m], hf[s][t], acc[c][t]);
        PIN(acc[c][t]);
      }
    }
    SBAR();
  });
  STAMP(3);
  __syncthreads();
  f32x4 v[9];
  {
    f32x4* ex = (f32x4*)smem + lane;
#pragma unroll
    for (int c = 0; c < 9; ++c) { ex[(w * 9 + c) * 64] = kh ? acc[c][0] : acc[c][1]; v[c] = kh ? acc[c][1] : acc[c][0]; }
    __syncthreads();
#pragma unroll
    for (int c = 0; c < 9; ++c) v[c] += ex[((w ^ 4) * 9 + c) * 64];
  }
  STAMP(4);
  const int grp = mt * 4 + cq;
  const __amdgpu_buffer_rsrc_t rx = __builtin_amdgcn_make_buffer_rsrc((void*)(ws + OFF_EX + (size_t)grp * 16 * 10240), 0, 16 * 10240, 0x00020000);
#pragma unroll
  for (int d = 0; d < 3; ++d) {
    const u32x4 pv = {pk2(v[2 * d][0], v[2 * d + 1][0]), pk2(v[2 * d][1], v[2 * d + 1][1]), pk2(v[2 * d][2], v[2 * d + 1][2]), pk2(v[2 * d][3], v[2 * d + 1][3])};
    __builtin_amdgcn_raw_buffer_store_b128(pv, rx, l16, ((((kq + 1 + d) & 3) * 4 + kq) * 10 + w) * 1024, 16);
  }
  if (tg != kq) __builtin_amdgcn_raw_buffer_store_b128(__builtin_bit_cast(u32x4, v[8]), rx, l16, ((tg * 4 + kq) * 10 + 8 + kh) * 1024, 16);
  wait_vm_bar<0>();
  STAMP(5);
  if (tid == 0) {
    unsigned* fl = (unsigned*)(ws + OFF_FL + (grp * 2 + (FINAL ? 1 : 0)) * 128);
    __hip_atomic_fetch_add(fl, 1u, __ATOMIC_RELAXED, __HIP_MEMORY_SCOPE_AGENT);
    while (__hip_atomic_load(fl, __ATOMIC_RELAXED, __HIP_MEMORY_SCOPE_AGENT) < 4u) __builtin_amdgcn_s_sleep(1);
  }
  wait_vm_bar<0>();
  STAMP(6);
  u32x4 in[3], in8[3];
#pragma unroll
  for (int d = 0; d < 3; ++d) in[d] = __builtin_amdgcn_raw_buffer_load_b128(rx, l16, ((kq * 4 + ((kq + 1 + d) & 3)) * 10 + w) * 1024, 16);
  if (tg == kq) {
#pragma unroll
    for (int d = 0; d < 3; ++d) in8[d] = __builtin_amdgcn_raw_buffer_load_b128(rx, l16, ((kq * 4 + ((kq + 1 + d) & 3)) * 10 + 8 + kh) * 1024, 16);
  }
  PIN(res); PIN(res8a); PIN(res8b);
  const u32x4 rv = __builtin_bit_cast(u32x4, res);
  u32x4 ov;
#pragma unroll
  for (int i = 0; i < 4; ++i) {
    float lo = v[6][i], hi = v[7][i];
#pragma unroll
    for (int d = 0; d < 3; ++d) { lo += bflo(in[d][i]); hi += bfhi(in[d][i]); }
    ov[i] = pk2(lo + bflo(rv[i]), hi + bfhi(rv[i]));
  }
  if (FINAL) *(u32x4*)(out + (size_t)tok * (D_ * 2) + (cq * 18 + kq * 4 + g) * 16) = ov;
  else st16(out + (cq * 18 + kq * 4 + g) * 512 + xoff, ov);
  f32x2 st = stats8(ov);
  if (tg == kq) {
    f32x4 a = v[8];
#pragma unroll
    for (int d = 0; d < 3; ++d) a += __builtin_bit_cast(f32x4, in8[d]);
    const u32x2 o8 = {pk2(a[0] + bflo(res8a), a[1] + bfhi(res8a)), pk2(a[2] + bflo(res8b), a[3] + bfhi(res8b))};
    if (FINAL) *(u32x2*)(out + (size_t)tok * (D_ * 2) + (cq * 18 + 16) * 16 + g * 8) = o8;
    else *(u32x2*)(out + (cq * 18 + 16 + (g >> 1)) * 512 + (g & 1) * 8 + xoff) = o8;
    st += stats8(u32x4{o8[0], o8[1], 0u, 0u});
  }
  if (!FINAL) {
    st[0] += __shfl_xor(st[0], 16); st[1] += __shfl_xor(st[1], 16);
    st[0] += xhalf(st[0]); st[1] += xhalf(st[1]);
    if (lane < 16) *(f32x2*)(ws + OFF_STA + (size_t)tok * 128 + (cq * 4 + kq) * 8) = st;
  }
  PROF_END(ws, 4);
}

extern "C" void kernel_launch(void* const* d_in, const int* in_sizes, int n_in, void* d_out, int out_size, void* d_ws,
                              size_t ws_size, hipStream_t stream) {
  const char* x = (const char*)d_in[0];
  const char* wqkv = (const char*)d_in[1];
  const char* wo = (const char*)d_in[2];
  const char* w1 = (const char*)d_in[4];
  const char* w3 = (const char*)d_in[5];
  const char* w2 = (const char*)d_in[6];
  char* ws = (char*)d_ws;
  constexpr int NWO = 6;
  constexpr int LDS_O = NWO * 64 * 76 * 4 + 4608;
  (void)hipFuncSetAttribute((const void*)k_qkv, hipFuncAttributeMaxDynamicSharedMemorySize, QKV_LDS);
  (void)hipFuncSetAttribute((const void*)k_attn<true>, hipFuncAttributeMaxDynamicSharedMemorySize, ATT_LDS);
  (void)hipFuncSetAttribute((const void*)k_attn<false>, hipFuncAttributeMaxDynamicSharedMemorySize, ATT_LDS);
  (void)hipFuncSetAttribute((const void*)k_up, hipFuncAttributeMaxDynamicSharedMemorySize, UP_LDS);
  (void)hipFuncSetAttribute((const void*)k_out<18, NWO, 3, false>, hipFuncAttributeMaxDynamicSharedMemorySize, LDS_O);
  (void)hipFuncSetAttribute((const void*)k_down<false>, hipFuncAttributeMaxDynamicSharedMemorySize, DN_LDS);
  (void)hipFuncSetAttribute((const void*)k_down<true>, hipFuncAttributeMaxDynamicSharedMemorySize, DN_LDS);
  k_prep<<<NJ_ALL, 64, 0, stream>>>(x, wqkv, ws);
  for (int l = 0; l < 2; ++l) {
    k_qkv<<<256, 256, QKV_LDS, stream>>>(ws + OFF_XA, ws);
    if (l == 0) k_attn<true><<<256, 512, ATT_LDS, stream>>>(ws, w1, w3, w2, wo);
    else k_attn<false><<<256, 512, ATT_LDS, stream>>>(ws, w1, w3, w2, wo);
    k_out<18, NWO, 3, false><<<256, NWO * 64, LDS_O, stream>>>(ws + OFF_OT, ws + OFF_WO, ws + OFF_XA, ws + OFF_XB, ws + OFF_STB);
    k_up<<<256, 512, UP_LDS, stream>>>(ws + OFF_XB, ws);
    if (l == 0) k_down<false><<<256, 512, DN_LDS, stream>>>(ws, ws + OFF_XA);
    else k_down<true><<<256, 512, DN_LDS, stream>>>(ws, (char*)d_out);
  }
}
```

```cpp
#include <hip/hip_runtime.h>
#include <hip/hip_bf16.h>
#include <stdint.h>

typedef short bf16x8 __attribute__((ext_vector_type(8)));
typedef short s16x4 __attribute__((ext_vector_type(4)));
typedef float f32x16 __attribute__((ext_vector_type(16)));
typedef float f32x4 __attribute__((ext_vector_type(4)));
typedef unsigned u32x4 __attribute__((ext_vector_type(4)));
typedef unsigned u32x2 __attribute__((ext_vector_type(2)));
typedef __bf16 bf2_t __attribute__((ext_vector_type(2)));
typedef float f32x2 __attribute__((ext_vector_type(2)));
#define LAS __attribute__((address_space(3)))
#define DI __device__ __forceinline__

constexpr int S_ = 2048, D_ = 576, H_ = 32, DH_ = 18, F_ = 4608;
constexpr int KS16 = D_ / 16;
constexpr size_t SZ_WQ = 64u * 36 * 1024, SZ_WO = 8u * 18 * 4608, SZ_W13 = 288u * 36 * 1024, SZ_W2 = 8u * 144 * 4608;
constexpr size_t SZ_CSQ = 9u * 64 * 32 * 4, SZ_CS13 = 9u * 288 * 32 * 4, SZ_X = (size_t)S_ * D_ * 2;
constexpr int KV_HALF = 77824, KV_HEAD = 2 * KV_HALF, KV_K2 = 32768, KV_V = 36864;
constexpr size_t SZ_KV = (size_t)H_ * KV_HEAD, SZ_QT = (size_t)H_ * 65536, SZ_Q2 = (size_t)H_ * 8192, SZ_HT = (size_t)S_ * F_ * 2;
constexpr size_t OFF_WQ = 0, OFF_WO = OFF_WQ + SZ_WQ, OFF_W13 = OFF_WO + SZ_WO, OFF_W2 = OFF_W13 + SZ_W13,
                 OFF_CSQ = OFF_W2 + SZ_W2, OFF_CS13 = OFF_CSQ + SZ_CSQ, OFF_XA = OFF_CS13 + SZ_CS13, OFF_XB = OFF_XA + SZ_X,
                 OFF_KV = OFF_XB + SZ_X, OFF_QT = OFF_KV + SZ_KV, OFF_Q2 = OFF_QT + SZ_QT, OFF_OT = OFF_Q2 + SZ_Q2,
                 OFF_HT = OFF_OT + SZ_X, OFF_STA = OFF_HT + SZ_HT, OFF_STB = OFF_STA + S_ * 128,
                 OFF_EX = OFF_STB + S_ * 128, WS_TOTAL = OFF_EX + 64u * 4 * 4 * 10240;

#define MFMA32(a, b, c) __builtin_amdgcn_mfma_f32_32x32x16_bf16(a, b, c, 0, 0, 0)
#define MFMA16(a, b, c) __builtin_amdgcn_mfma_f32_16x16x32_bf16(a, b, c, 0, 0, 0)
#define SBAR() __builtin_amdgcn_sched_barrier(0)
#define PIN(x) asm volatile("" : "+v"(x))

#define PROF_DECL
#define STAMP(i)
#define PROF_END(wsp, kid)
DI unsigned pk2(float lo, float hi) { f32x2 v = {lo, hi}; return __builtin_bit_cast(unsigned, __builtin_convertvector(v, bf2_t)); }
DI float bflo(unsigned u) { return __uint_as_float(u << 16); }
DI float bfhi(unsigned u) { return __uint_as_float(u & 0xffff0000u); }
DI float xhalf(float v) {
  auto rr = __builtin_amdgcn_permlane32_swap(__float_as_uint(v), __float_as_uint(v), false, false);
  return (threadIdx.x & 32) ? __uint_as_float(rr[0]) : __uint_as_float(rr[1]);
}
DI void set_m0(unsigned lds_base) { asm volatile("s_mov_b32 m0, %0" ::"s"(lds_base) : "memory"); }
template <int OFF> DI void glds(const char* base, unsigned voff) {
  asm volatile("global_load_lds_dwordx4 %0, %1 offset:%2" ::"v"(voff), "s"(base), "i"(OFF) : "memory");
}
template <int OFF> DI void glds16(const char* base, unsigned voff, unsigned lds_base) {
  asm volatile("s_mov_b32 m0, %2\n\ts_nop 0\n\tglobal_load_lds_dwordx4 %0, %1 offset:%3" ::"v"(voff), "s"(base), "s"(lds_base), "i"(OFF) : "memory");
}
template <int OFF> DI void gload16(bf16x8& d, const char* base, unsigned voff) {
  asm volatile("global_load_dwordx4 %0, %1, %2 offset:%3" : "=v"(d) : "v"(voff), "s"(base), "i"(OFF) : "memory");
}
template <int OFF> DI void bload16(bf16x8& d, const __amdgpu_buffer_rsrc_t& rs, unsigned voff, int soff) {
  asm volatile("buffer_load_dwordx4 %0, %1, %2, %3 offen offset:%4" : "=v"(d) : "v"(voff), "s"(rs), "s"(soff), "i"(OFF) : "memory");
}
DI void gload4(unsigned& d, const char* base, unsigned voff) {
  asm volatile("global_load_dword %0, %1, %2" : "=v"(d) : "v"(voff), "s"(base) : "memory");
}
#define WT 1
DI void st16(char* p, const u32x4& v) { if (WT) asm volatile("global_store_dwordx4 %0, %1, off sc1\n\ts_nop 1" ::"v"(p), "v"(v) : "memory"); else *(u32x4*)p = v; }
DI void st8(char* p, const u32x2& v) { asm volatile("global_store_dwordx2 %0, %1, off sc1" ::"v"(p), "v"(v) : "memory"); }
DI void st8(char* p, const f32x2& v) { st8(p, __builtin_bit_cast(u32x2, v)); }
DI void st4(char* p, float v) { asm volatile("global_store_dword %0, %1, off sc1" ::"v"(p), "v"(v) : "memory"); }
#define PWT 1
DI void PST(char* base, unsigned off, const u32x4& v) {
  if (PWT) asm volatile("global_store_dwordx4 %0, %1, %2 sc1\n\ts_nop 1" ::"v"(off), "v"(v), "s"(base) : "memory"); else *(u32x4*)(base + off) = v;
}
template <int N> DI void wait_vm() { asm volatile("s_waitcnt vmcnt(%0)" ::"i"(N) : "memory"); }
template <int N> DI void wait_vm_bar() { asm volatile("s_waitcnt vmcnt(%0)\n\ts_barrier" ::"i"(N) : "memory"); }
template <int N> DI void wait_vm_lgkm_bar() { asm volatile("s_waitcnt vmcnt(%0) lgkmcnt(0)\n\ts_barrier" ::"i"(N) : "memory"); }
template <int I> struct IC { static constexpr int v = I; };
template <int B, int E, class Fn> DI void sfor(Fn&& f) { if constexpr (B < E) { f(IC<B>{}); sfor<B + 1, E>(f); } }
DI bf16x8 ldsr(unsigned addr) { return *(const LAS bf16x8*)addr; }
#define HAUX 16
#define DSLEEP 2
#define WIT 4
#define ASLEEP 4
#define FWT 0
#define ROT 0
DI int rot9(int v) { return v >= 9 ? v - 9 : v; }
DI int opaque(int v) { asm volatile("" : "+s"(v)); return v; }

DI u32x4 not_empty(u32x4 v) {
#pragma unroll
  for (int i = 0; i < 4; ++i) v[i] = min(v[i], 0xfffffffeu);
  return v;
}
DI unsigned max4(const u32x4& v) { return max(max(v[0], v[1]), max(v[2], v[3])); }
template <int OFF> DI void mark_empty(const char* base, unsigned voff) {
  asm volatile("global_store_dwordx4 %0, %1, %2 offset:%3 sc1\n\ts_nop 1" ::"v"(voff), "v"(u32x4{~0u, ~0u, ~0u, ~0u}), "s"(base), "i"(OFF) : "memory");
}
DI f32x2 stats8(const u32x4& v) {
  float s1 = 0.f, s2 = 0.f;
#pragma unroll
  for (int i = 0; i < 4; ++i) { const float lo = bflo(v[i]), hi = bfhi(v[i]); s1 += lo + hi; s2 = fmaf(lo, lo, fmaf(hi, hi, s2)); }
  return f32x2{s1, s2};
}
constexpr int NJW = 648 * 3;
struct WJob { int kind, kb, nb; };
DI WJob wjob(int job) {
  WJob j;
  if (job < 1296) { j.kind = job >= 648; const int q = job - j.kind * 648; j.kb = q / 72; j.nb = q % 72; }
  else { j.kind = 2; const int q = job - 1296; j.kb = q / 9; j.nb = q % 9; }
  return j;
}
struct WLoad { __amdgpu_buffer_rsrc_t rs; unsigned voff; int soff, st[5]; };
DI WLoad wjob_src(const WJob& j, int lane, const char* src) {
  const int ld = j.kind < 2 ? F_ * 2 : j.kind == 4 ? 3 * D_ * 2 : D_ * 2;
  int r0 = j.kb * 64, rk = 8, ra = 16, rb = 32, j0 = 1, j1 = 2, j2 = 4;
  if (j.kind == 2) { ra = 1; j0 = 2; j1 = 4; j2 = 16; }
  if (j.kind == 3) {
    if (j.kb < 8) { r0 = j.kb * 72; rk = 1; ra = 18; rb = 36; j0 = 2; j1 = 4; j2 = 8; }
    else { r0 = 16; rk = 72; ra = 144; rb = 288; j1 = 18; j2 = 36; }
  }
  return WLoad{__builtin_amdgcn_make_buffer_rsrc((void*)(src + (size_t)r0 * ld + j.nb * 128), 0, 1 << 20, 0x00020000),
               (unsigned)((lane >> 5) * rk * ld + (lane & 31) * 4), 0, {j0 * ld, j1 * ld, j2 * ld, ra * ld, rb * ld}};
}
template <int K> DI void wjob_load(WLoad& l, unsigned (&r)[32]) {
  constexpr int i = K ^ (K >> 1), b = K ? __builtin_ctz(K | 32) : 0;
  if constexpr (K > 0) {
    if constexpr ((i >> b) & 1) asm volatile("s_add_i32 %0, %0, %1" : "+s"(l.soff) : "s"(l.st[b]) : "scc");
    else asm volatile("s_sub_i32 %0, %0, %1" : "+s"(l.soff) : "s"(l.st[b]) : "scc");
  }
  asm volatile("buffer_load_dword %0, %1, %2, %3 offen" : "=v"(r[i]) : "v"(l.voff), "s"(l.rs), "s"(l.soff) : "memory");
}
struct WStore { unsigned off, es, p1, p2, cso; f32x16 ca[2]; };
DI WStore wjob_dst(const WJob& j, int lane) {
  const int kg = lane >> 5, cp = lane & 31, n = j.nb * 64 + cp * 2;
  unsigned off, es, p1 = 1024, p2 = 2048, cso = 0, ce = 0;
  if (j.kind < 2) {
    const int chunk = j.nb * 4 + (cp >> 4) * 2, rho = cp & 15;
    off = OFF_W13 + ((chunk * 72 + j.kb * 8 + kg) * 32 + j.kind * 16 + rho) * 16; es = 36864;
    const int ck = chunk + kg;
    cso = OFF_CS13 + ((ck + ck / 9 * 72 + j.kb * 9) * 32 + ((rho >> 2) & 1) * 16 + j.kind * 8 + (rho >> 3) * 4 + (rho & 3)) * 4;
  } else if (j.kind == 2) {
    const int cq = n / 144, cc = n % 144, s = (j.kb % 9) * 2, pr = ((cc >> 5) - j.kb / 18 - 1) & 3;
    off = OFF_W2 + (cq * 8 + j.kb / 9) * 162 * 1024 + kg * 256; p1 = 512; p2 = 7168;
    if (cc >= 128) { off += (s * 7 + 6) * 1024 + (cc - 128) * 16; es = 16; }
    else {
      off += (cc & 31) * 8; es = 1024;
      if (pr < 3) off += (s * 7 + pr * 2) * 1024; else { off += (126 + s * 2) * 1024; p2 = 2048; }
    }
  } else if (j.kind == 3) {
    const int cq = n / 144, cc = n % 144;
    off = OFF_WO + (cq * 18 + j.kb * 2) * 9216 + kg * 256; p1 = 512; p2 = 9216;
    if (cc < 128) { off += (cc >> 5) * 2048 + (cc & 31) * 8; es = 1024; }
    else { off += 8192 + (cc - 128) * 16; es = 16; }
  } else {
    const int part = n / D_, col = n % D_, h = col / DH_, d = col % DH_;
    int chunk, rho;
    if (d < 16) { chunk = part == 2 ? 2 * h + 1 : 2 * h; rho = (part == 0 ? 16 : 0) + (d >> 1); es = 128; ce = 16; }
    else { chunk = 2 * h + 1; rho = part == 0 ? 24 : 20 - 2 * part; es = 16; ce = 4; }
    off = OFF_WQ + ((chunk * 72 + j.kb * 8 + kg) * 32 + rho) * 16;
    cso = OFF_CSQ + ((j.kb * 64 + chunk) * 32 + ((rho >> 2) & 1) * 16 + (rho >> 3) * 4 + (rho & 3)) * 4;
  }
  WStore d;
  d.off = off; d.es = es; d.p1 = p1; d.p2 = p2; d.cso = cso + kg * ce;
  return d;
}
template <int K> DI void wjob_store(WStore& d, int lane, const unsigned (&r)[32], char* ws) {
  if constexpr (K < 8) {
    constexpr int g = K ^ (K >> 1), e = g & 1, pp = g >> 1;
    u32x4 v;
#pragma unroll
    for (int q = 0; q < 4; ++q) v[q] = __builtin_amdgcn_perm(r[pp * 8 + 2 * q + 1], r[pp * 8 + 2 * q], e ? 0x07060302u : 0x05040100u);
    if constexpr (K > 0) d.off += K == 2 ? d.p1 : K == 4 ? d.p2 : K == 6 ? -d.p1 : e ? d.es : -d.es;
    PST(ws, d.off, v);
    const u32x4 one = {0x3f803f80u, 0x3f803f80u, 0x3f803f80u, 0x3f803f80u};
    d.ca[e] = MFMA32(__builtin_bit_cast(bf16x8, one), __builtin_bit_cast(bf16x8, v), K > 1 ? d.ca[e] : (f32x16){});
    PIN(d.ca[e]);
  } else {
    st4(ws + d.cso, lane >= 32 ? d.ca[1][0] : d.ca[0][0]);
  }
}

constexpr int NJ_Q = 36 * 9, NJ_X = 256, NJ_Z = 32, NJ_ALL = NJ_Q + NJ_X + NJ_Z;
__global__ __launch_bounds__(64) void k_prep(const char* __restrict__ x, const char* __restrict__ wqkv, const char* __restrict__ wo,
                                             char* __restrict__ ws) {
  const int lane = threadIdx.x, job = blockIdx.x;
  if (job < NJ_Q) {
    const int nb = job % 36;
    const WJob j = {nb < 27 ? 4 : 3, job / 36, nb < 27 ? nb : nb - 27};
    unsigned r[32];
    WLoad l = wjob_src(j, lane, nb < 27 ? wqkv : wo);
    sfor<0, 32>([&](auto k) { wjob_load<decltype(k)::v>(l, r); });
    WStore d = wjob_dst(j, lane);
    wait_vm<0>();
#pragma unroll
    for (int i = 0; i < 32; ++i) PIN(r[i]);
    sfor<0, 8>([&](auto k) { wjob_store<decltype(k)::v>(d, lane, r, ws); });
    if (nb < 27) wjob_store<8>(d, lane, r, ws);
  } else if (job < NJ_Q + NJ_X) {
    const int tok = (job - NJ_Q) * 8 + (lane & 7), pg = lane >> 3;
    u32x4 v[9];
#pragma unroll
    for (int i = 0; i < 9; ++i) v[i] = *(const u32x4*)(x + (size_t)tok * (D_ * 2) + (i * 8 + pg) * 16);
    f32x2 a = {0.f, 0.f};
#pragma unroll
    for (int i = 0; i < 9; ++i) {
      st16(ws + OFF_XA + ((size_t)((tok >> 5) * 72 + i * 8 + pg) * 32 + (tok & 31)) * 16, v[i]);
      a += stats8(v[i]);
    }
    st16(ws + OFF_STA + (tok >> 5) * 4096 + pg * 512 + (tok & 31) * 16, __builtin_bit_cast(u32x4, f32x4{a[0], a[1], 0.f, 0.f}));
  } else {
    const int h = job - NJ_Q - NJ_X, c = 2 * h + 1;
    for (int i = lane; i < 72 * 10; i += 64) {
      const int p = i / 10, q = i % 10, rho = q < 4 ? 20 + q : 22 + q;
      st16(ws + OFF_WQ + ((size_t)(c * 72 + p) * 32 + rho) * 16, u32x4{0, 0, 0, 0});
    }
  }
}

DI void stats_fin(const f32x4& a, const f32x4& b, float& mu, float& rstd, float& ve) {
  float s1 = (a[0] + a[2]) + (b[0] + b[2]), s2 = (a[1] + a[3]) + (b[1] + b[3]);
  s1 += xhalf(s1); s2 += xhalf(s2);
  mu = s1 * (1.f / D_);
  const float var = fmaxf(s2 * (1.f / D_) - mu * mu, 0.f);
  ve = var + 1e-5f;
  rstd = __builtin_amdgcn_rsqf(ve);
}

constexpr int QKV_ST = 4 * 36 * 1024 + 512, QKV_LDS = QKV_ST + 8 * 1536;
__global__ __launch_bounds__(256, 2) void k_qkv(const char* __restrict__ xt, char* __restrict__ ws) {
  extern __shared__ __attribute__((aligned(16))) char smem[];
  const int tid = threadIdx.x, lane = tid & 63, hh = lane >> 5, w = __builtin_amdgcn_readfirstlane(tid >> 6);
  const int xcd = blockIdx.x & 7, idx = blockIdx.x >> 3;
  const int mt = (xcd & 3) * 4 + (idx & 3), hp = (xcd >> 2) * 8 + (idx >> 2);
  const int tt = mt * 4 + w;
  PROF_DECL;
  const unsigned lds0 = (unsigned)(uintptr_t)smem;
  const char* wsrc = ws + OFF_WQ + (size_t)(hp * 4 + w) * 36 * 1024;
  const char* xsrc = xt + (size_t)tt * 36 * 1024;
  const unsigned l16 = lane * 16;
  float* csl = (float*)(smem + 4 * 36 * 1024);
  bf16x8 xf[36];
  constexpr int QD = 4;
  const int rot = ROT ? rot9((idx & 3) * 2 + (idx >> 2)) : 0;
  auto issue = [&](auto ic) {
    constexpr int s = decltype(ic)::v >> 1;
    if constexpr (s < 36) {
      const int o = rot9((s >> 2) + rot) * 4096;
      if constexpr (decltype(ic)::v & 1) glds<(s & 3) * 1024>(wsrc + o, l16);
      else {
        if constexpr ((s & 3) == 0) set_m0(lds0 + w * 36864 + (s >> 2) * 4096);
        gload16<(s & 3) * 1024>(xf[s], xsrc + o, l16);
      }
    }
  };
  sfor<0, 6 * QD>(issue);
  unsigned csr[9];
#pragma unroll
  for (int i = 0; i < 9; ++i) gload4(csr[i], ws + OFF_CSQ + (hp * 128 + i * 2048) * 4, (tid & 127) * 4);
  bf16x8 sta, stb, stc, std_;
  gload16<0>(sta, ws + OFF_STA + tt * 4096, l16);
  gload16<1024>(stb, ws + OFF_STA + tt * 4096, l16);
  gload16<2048>(stc, ws + OFF_STA + tt * 4096, l16);
  gload16<3072>(std_, ws + OFF_STA + tt * 4096, l16);
  f32x16 acc[4] = {};
  unsigned lb[4];
#pragma unroll
  for (int c = 0; c < 4; ++c) { lb[c] = lds0 + l16 + c * 36864; PIN(lb[c]); }
  constexpr float QS = 0.23570226039551584f * 1.4426950408889634f;
  float rstd, rsq, nmr, nmq;
  f32x4 cm[16];
  constexpr int PD = 6;
  bf16x8 ring[144];
  sfor<0, 144 + PD>([&](auto jc) {
    constexpr int r = decltype(jc)::v, m = r - PD;
    if constexpr (r < 144 && r % 12 == 0) {
      constexpr int g = r / 12;
      wait_vm_bar<6 * (g + QD <= 12 ? QD - 1 : 11 - g) + (g < QD ? 13 : 0)>();
      if constexpr (g % 3 == 0) STAMP(1 + g / 3);
      if constexpr (g == QD) {
        float cst = 0.f;
#pragma unroll
        for (int i = 0; i < 9; ++i) { PIN(csr[i]); cst += __uint_as_float(csr[i]); }
        csl[tid & 127] = cst;
      }
#pragma unroll
      for (int s = 3 * g; s < 3 * g + 3; ++s) PIN(xf[s]);
    }
    if constexpr (r < 144 && r % 2 == 0) issue(IC<6 * QD + r / 2>{});
    if constexpr (r == 98) {
      float mu, ve;
      PIN(sta); PIN(stb); PIN(stc); PIN(std_);
      stats_fin(__builtin_bit_cast(f32x4, sta) + __builtin_bit_cast(f32x4, stc), __builtin_bit_cast(f32x4, stb) + __builtin_bit_cast(f32x4, std_), mu, rstd, ve);
      nmr = -mu * rstd; rsq = rstd * QS; nmq = nmr * QS;
    }
    if constexpr (r >= 100 && r < 132 && (r & 1) == 0) cm[(r - 100) / 2] = *(const f32x4*)(csl + (r - 100) / 8 * 32 + hh * 16 + (r - 100) / 2 % 4 * 4);
    if constexpr (r >= 107 && r < 139 && (r & 1)) {
      constexpr int i = (r - 107) / 2;
      const float f = (i & 7) == 2 || (i & 7) == 3 || (i & 7) == 7 ? nmq : nmr;
#pragma unroll
      for (int e = 0; e < 4; ++e) { float t = cm[i][e] * f; PIN(t); cm[i][e] = t; }
    }
    if constexpr (r == 134 || r == 137 || r == 140) mark_empty<(r - 134) / 3 * 1024>(ws + OFF_OT + (size_t)blockIdx.x * 9216 + w * 2048, l16);
    if constexpr (r < 144) ring[r] = ldsr(lb[r & 3] + (r >> 2) * 1024);
    if constexpr (m >= 0) {
      acc[m & 3] = MFMA32(ring[m], xf[m >> 2], acc[m & 3]);
      PIN(acc[m & 3]);
    }
    SBAR();
  });
  STAMP(7);
  const int tok = tt * 32 + (lane & 31);
#pragma unroll
  for (int hq = 0; hq < 2; ++hq) {
    const int head = hp * 2 + hq;
    float v[2][16];
#pragma unroll
    for (int c = 0; c < 2; ++c)
#pragma unroll
      for (int r4 = 0; r4 < 4; ++r4) {
#pragma unroll
        for (int i = 0; i < 4; ++i) v[c][r4 * 4 + i] = fmaf(acc[hq * 2 + c][r4 * 4 + i], r4 > 1 + c ? rsq : rstd, cm[hq * 8 + c * 4 + r4][i]);
      }
    char* kv = ws + OFF_KV + (size_t)head * KV_HEAD + (tok >> 10) * KV_HALF;
    st16(kv + (tt & 31) * 1024 + lane * 16, u32x4{pk2(v[0][0], v[0][1]), pk2(v[0][2], v[0][3]), pk2(v[0][4], v[0][5]), pk2(v[0][6], v[0][7])});
    st16(ws + OFF_QT + (size_t)head * 65536 + tt * 1024 + lane * 16,
         u32x4{pk2(v[0][8], v[0][9]), pk2(v[0][10], v[0][11]), pk2(v[0][12], v[0][13]), pk2(v[0][14], v[0][15])});
    const unsigned sb = lds0 + QKV_ST + (w * 2 + hq) * 1536, vr = sb + (lane & 31) * 40;
    *(LAS u32x2*)(vr + hh * 16) = u32x2{pk2(v[1][0], v[1][1]), pk2(v[1][2], v[1][3])};
    *(LAS u32x2*)(vr + hh * 16 + 8) = u32x2{pk2(v[1][4], v[1][5]), pk2(v[1][6], v[1][7])};
    if (hh == 0) {
      *(LAS u32x2*)(vr + 32) = u32x2{pk2(v[1][8], v[1][9]), 0x00003f80u};
      *(LAS unsigned*)(sb + 1280 + lane * 4) = pk2(v[1][10], v[1][11]);
      *(LAS unsigned*)(sb + 1408 + lane * 4) = pk2(v[1][12], v[1][13]);
    }
  }
  {
    const unsigned sb = lds0 + QKV_ST + w * 3072;
    const unsigned kvo = OFF_KV + hp * 2 * KV_HEAD + (tt >> 5) * KV_HALF, vo = kvo + KV_V + (tt & 31) * 1280;
    asm volatile("s_waitcnt lgkmcnt(0)" ::: "memory");
    const u32x4 a = *(const LAS u32x4*)(sb + l16), b = *(const LAS u32x4*)(sb + 1536 + l16);
    const u32x4 c = *(const LAS u32x4*)(sb + hh * 1536 + 1024 + (lane & 31) * 16);
    PST(ws + vo, l16, a);
    PST(ws + vo + KV_HEAD, l16, b);
    const int i = lane & 31;
    PST(ws, i < 16 ? vo + hh * KV_HEAD + 1024 + i * 16 : i < 24 ? kvo + hh * KV_HEAD + KV_K2 + (tt & 31) * 128 + (i - 16) * 16
                                                                 : (unsigned)OFF_Q2 + (hp * 2 + hh) * 8192 + tt * 128 + (i - 24) * 16, c);
  }
  PROF_END(ws, 0);
}

constexpr int ATT_LDS = KV_HEAD + 2048;
#define MFMA8(a, b, c) __builtin_amdgcn_mfma_f32_32x32x8bf16_1k(a, b, c, 0, 0, 0)
DI s16x4 vtr(unsigned p) { return __builtin_amdgcn_ds_read_tr16_b64_v4i16((LAS s16x4*)p); }
DI bf16x8 vtr2(unsigned p) { return __builtin_shufflevector(vtr(p), vtr(p + 320), 0, 1, 2, 3, 4, 5, 6, 7); }
DI float max16(const f32x16& s) {
  float a = fmaxf(fmaxf(s[0], s[1]), s[2]), b = fmaxf(fmaxf(s[3], s[4]), s[5]);
  a = fmaxf(fmaxf(a, s[6]), s[7]); b = fmaxf(fmaxf(b, s[8]), s[9]);
  a = fmaxf(fmaxf(a, s[10]), s[11]); b = fmaxf(fmaxf(b, s[12]), s[13]);
  a = fmaxf(fmaxf(a, s[14]), s[15]);
  return fmaxf(a, b);
}
DI s16x4 make_qb(unsigned q2, float m, int hh) {
  const float nm = -m;
  const float h0 = bfhi(__float_as_uint(nm)), r0 = nm - h0, h1 = bfhi(__float_as_uint(r0)), r1 = r0 - h1;
  u32x2 u = {q2, (__float_as_uint(h0) >> 16) | __float_as_uint(h1)};
  if (hh) u = u32x2{0u, __float_as_uint(r1) >> 16};
  return __builtin_bit_cast(s16x4, u);
}
DI void pin16(f32x16& v) { PIN(v); }
#define EX(i) e[i] = __builtin_amdgcn_exp2f(sc[i]); PIN(e[i])

template <bool FIRST>
__global__ __launch_bounds__(512, 2) void k_attn(char* __restrict__ ws, const char* __restrict__ w1, const char* __restrict__ w3,
                                                 const char* __restrict__ w2) {
  extern __shared__ __attribute__((aligned(16))) char smem[];
  const int tid = threadIdx.x, lane = tid & 63, hh = lane >> 5, w = __builtin_amdgcn_readfirstlane(tid >> 6);
  const int xcd = blockIdx.x & 7, idx = blockIdx.x >> 3;
  const int head = xcd * 4 + (idx & 3), qb = idx >> 2;
  const int qg = w & 3, kh = w >> 2;
  PROF_DECL;
  const unsigned lds0 = (unsigned)(uintptr_t)smem + kh * KV_HALF, l16 = lane * 16;
  const char* kvsrc = ws + OFF_KV + (size_t)head * KV_HEAD + kh * KV_HALF;
  bf16x8 qa[2]; unsigned q2[2];
#pragma unroll
  for (int q = 0; q < 2; ++q) {
    const int qt = qb * 8 + qg * 2 + q;
    gload16<0>(qa[q], ws + OFF_QT + (size_t)head * 65536 + qt * 1024, l16);
    gload4(q2[q], ws + OFF_Q2 + (size_t)head * 8192 + qt * 128, (lane & 31) * 4);
  }
#pragma unroll
  for (int i = 0; i < 5; ++i) {
    int p = qg * 5 + i; p = p > 18 ? 18 : p;
    const int off = (p < 8 ? p : p == 8 ? 32 : 27 + p) * 1024;
    glds16<0>(kvsrc + off, l16, lds0 + off);
  }
  auto late = [&](int i) {
    const int q = opaque(qg);
    const int off = (i < 6 ? 8 + 4 * i + q : i == 6 ? (q == 3 ? 46 : 33 + q) : i < 14 ? 19 + 4 * i + q : 75) * 1024;
    glds16<0>(kvsrc + off, l16, lds0 + off);
  };
  __builtin_amdgcn_s_setprio(1);
  const int i16 = lane & 15;
  const unsigned kp0 = lds0 + l16, k2p0 = lds0 + KV_K2 + (lane & 31) * 4;
  const unsigned vp0 = lds0 + KV_V + (4 * hh + (i16 >> 2)) * 40 + ((lane & 16) ? 32 : (i16 & 3) * 8);
  const f32x16 zero = {};
  f32x16 o[2];
  float m[2];
  u32x2 kb = {0u, 0x3f803f80u};
  PIN(kb);
  STAMP(1);
  wait_vm_bar<0>();
  STAMP(2);
  PIN(qa[0]); PIN(qa[1]); PIN(q2[0]); PIN(q2[1]);
  {
    const bf16x8 ka = ldsr(kp0);
    kb[0] = *(const LAS unsigned*)k2p0;
#pragma unroll
    for (int q = 0; q < 2; ++q) {
      f32x16 s = MFMA32(ka, qa[q], zero);
      s = MFMA8(__builtin_bit_cast(s16x4, kb), make_qb(q2[q], 0.f, hh), s);
      float mx = max16(s);
      m[q] = fmaxf(mx, xhalf(mx));
    }
  }
  bf16x8 wf[27];
  const __amdgpu_buffer_rsrc_t rwo = __builtin_amdgcn_make_buffer_rsrc((void*)(ws + OFF_WO + ((head & 3) * 18 + w * 3 - 6) * 9216), 0, w < 2 ? 0 : 27648, 0x00020000);
  auto wo_load = [&](auto ic) {
    constexpr int i = decltype(ic)::v;
    if constexpr (i < 27) bload16<(i & 3) * 1024>(wf[i], rwo, l16, (i >> 2) * 4096);
  };
  WLoad wl;
  WStore wd;
  unsigned wr[32];
  for (int pass = 0; pass < 2; ++pass) {
    const s16x4 qb2[2] = {make_qb(q2[0], m[0], hh), make_qb(q2[1], m[1], hh)};
    o[0] = zero; o[1] = zero;
    unsigned kp = kp0, k2p = k2p0, vp = vp0;
    PIN(kp); PIN(k2p); PIN(vp);
    bf16x8 ka = ldsr(kp);
    kb[0] = *(const LAS unsigned*)k2p;
    bf16x8 v0 = vtr2(vp), v1 = vtr2(vp + 640);
    f32x16 sa = MFMA32(ka, qa[0], zero), sb;
    sa = MFMA8(__builtin_bit_cast(s16x4, kb), qb2[0], sa);
    u32x4 p1 = {0u, 0u, 0u, 0u};
    float ep[4] = {0.f, 0.f, 0.f, 0.f};
    auto half = [&](auto qc, auto tc, const f32x16& sc, f32x16& sn) {
      constexpr int Q = decltype(qc)::v, T = decltype(tc)::v;
      float e[16]; u32x4 p0;
      sn = MFMA32(ka, qa[Q ^ 1], zero);
      SBAR();
      if constexpr (Q == 0) ka = ldsr(kp + (T + 1) * 1024);
      p1[2] = pk2(ep[0], ep[1]); p1[3] = pk2(ep[2], ep[3]); PIN(p1); EX(0); EX(1); EX(2); EX(3);
      SBAR();
      o[Q ^ 1] = MFMA32(v1, __builtin_bit_cast(bf16x8, p1), o[Q ^ 1]);
      SBAR();
      if constexpr (Q == 0) v1 = vtr2(vp + T * 1280 + 640);
      p0[0] = pk2(e[0], e[1]); p0[1] = pk2(e[2], e[3]); SBAR(); EX(4); EX(5); EX(6); EX(7);
      SBAR();
      sn = MFMA8(__builtin_bit_cast(s16x4, kb), qb2[Q ^ 1], sn);
      SBAR();
      if constexpr (Q == 0) kb[0] = *(const LAS unsigned*)(k2p + (T + 1) * 128);
      p0[2] = pk2(e[4], e[5]); p0[3] = pk2(e[6], e[7]); PIN(p0); EX(8); EX(9); EX(10); EX(11);
      SBAR();
      o[Q] = MFMA32(v0, __builtin_bit_cast(bf16x8, p0), o[Q]);
      SBAR();
      if constexpr (Q == 1) v0 = vtr2(vp + (T + 1) * 1280);
      p1[0] = pk2(e[8], e[9]); p1[1] = pk2(e[10], e[11]); SBAR(); EX(12); EX(13); EX(14); EX(15);
      ep[0] = e[12]; ep[1] = e[13]; ep[2] = e[14]; ep[3] = e[15];
      SBAR();
    };
    auto iter = [&](auto mc) {
      constexpr int IT = decltype(mc)::v;
      sfor<0, 4>([&](auto tc) {
        constexpr int T = decltype(tc)::v;
        if constexpr (T == 0) asm volatile("s_cmp_eq_u32 %0, 0\n\ts_cbranch_scc1 1f\n\ts_setprio 2\n1:" ::"s"(kh) : "scc");
        if constexpr (T == 3) asm volatile("s_cmp_eq_u32 %0, 0\n\ts_cbranch_scc1 1f\n\ts_setprio 0\n1:" ::"s"(kh) : "scc");
        if constexpr (IT == 0) { late(4 * T); late(4 * T + 1); }
        if constexpr (IT == 1 && T == 3) { STAMP(3); wait_vm_bar<FIRST ? 6 : 0>(); STAMP(4); }
        if constexpr (FIRST && IT >= 3 && IT < 7 && T == 0) {
          wait_vm<IT == 3 ? 12 : IT < 6 ? 10 : 2>();
#pragma unroll
          for (int i = 0; i < 8; ++i) PIN(wr[((IT - 3) ^ ((IT - 3) >> 1)) * 8 + i]);
        }
        if constexpr (FIRST && IT >= 1 && IT < 5) wjob_load<IT * 8 - 8 + T * 2>(wl, wr);
        if constexpr (IT == 6 || IT == 7) { wo_load(IC<(IT - 6) * 16 + T * 4>{}); wo_load(IC<(IT - 6) * 16 + T * 4 + 1>{}); }
        SBAR();
        half(IC<0>{}, tc, sa, sb);
        if constexpr (IT == 0) { late(4 * T + 2); if (T < 3) late(4 * T + 3); }
        if constexpr (FIRST && IT >= 1 && IT < 5) wjob_load<IT * 8 - 7 + T * 2>(wl, wr);
        if constexpr (FIRST && IT >= 3 && IT < 7 && (T & 1) == 0) wjob_store<(IT - 3) * 2 + T / 2>(wd, lane, wr, ws);
        if constexpr (IT == 2)
          mark_empty<T * 1024>(ws + OFF_EX + (size_t)blockIdx.x * 40960 + min(w * 4, 26) * 1024, l16);
        if constexpr (IT == 6 || IT == 7) { wo_load(IC<(IT - 6) * 16 + T * 4 + 2>{}); wo_load(IC<(IT - 6) * 16 + T * 4 + 3>{}); }
        SBAR();
        half(IC<1>{}, tc, sb, sa);
      });
      kp += 4096; k2p += 512; vp += 5120;
      pin16(o[0]); pin16(o[1]);
    };
    if (__builtin_expect(pass == 0, 1)) {
      iter(IC<0>{}); STAMP(6);
      const int job = blockIdx.x * 8 + opaque(w);
      const WJob wj = wjob(job < NJW ? job : job - NJW);
      if (FIRST) wl = wjob_src(wj, lane, wj.kind == 0 ? w1 : wj.kind == 1 ? w3 : w2);
      iter(IC<1>{}); STAMP(7); iter(IC<2>{});
      if (FIRST) {
        int ln = lane; PIN(ln);
        wd = wjob_dst(wj, ln);
        STAMP(8);
        iter(IC<3>{}); iter(IC<4>{}); iter(IC<5>{});
      } else {
        for (int it = 3; it < 6; ++it) iter(IC<9>{});
      }
      iter(IC<6>{});
      if (FIRST && wj.kind < 2) wjob_store<8>(wd, lane, wr, ws);
      iter(IC<7>{});
    } else {
      for (int it = 0; it < 8; ++it) iter(IC<9>{});
      sfor<0, 27>(wo_load);
    }
    wait_vm<0>();
#pragma unroll
    for (int i = 0; i < 27; ++i) PIN(wf[i]);
    p1[2] = pk2(ep[0], ep[1]); p1[3] = pk2(ep[2], ep[3]);
    o[1] = MFMA32(v1, __builtin_bit_cast(bf16x8, p1), o[1]);
    bool ok = true;
#pragma unroll
    for (int q = 0; q < 2; ++q) {
      float a = o[q][10];
#pragma unroll
      for (int r = 0; r < 10; ++r) a = fmaxf(a, fabsf(o[q][r]) * 0x1p-40f);
      ok = ok && (a < 0x1p60f);
#pragma unroll
      for (int r = 0; r < 11; ++r) ok = ok && (o[q][r] == o[q][r]);
    }
    if (__builtin_expect(pass == 1 || __all(ok), 1)) break;
    const s16x4 qz0 = make_qb(q2[0], 0.f, hh), qz1 = make_qb(q2[1], 0.f, hh);
    float mx0 = m[0], mx1 = m[1];
    for (int t = 0; t < 32; ++t) {
      const bf16x8 kk = ldsr(kp0 + t * 1024);
      kb[0] = *(const LAS unsigned*)(k2p0 + t * 128);
      f32x16 s = MFMA32(kk, qa[0], zero); s = MFMA8(__builtin_bit_cast(s16x4, kb), qz0, s); mx0 = fmaxf(mx0, max16(s));
      s = MFMA32(kk, qa[1], zero); s = MFMA8(__builtin_bit_cast(s16x4, kb), qz1, s); mx1 = fmaxf(mx1, max16(s));
    }
    m[0] = fmaxf(mx0, xhalf(mx0)); m[1] = fmaxf(mx1, xhalf(mx1));
  }
  STAMP(5);
  __builtin_amdgcn_s_setprio(0);
  const int cq = head & 3, t32 = qb * 8 + (head >> 2), g = lane >> 4;
  const unsigned xoff = t32 * 72 * 512 + ((w & 1) * 16 + (lane & 15)) * 16;
  bf16x8 res; unsigned res8a, res8b;
  gload16<0>(res, ws + OFF_XA + (cq * 18 + (w >> 1) * 4) * 512, xoff + g * 512);
  gload4(res8a, ws + OFF_XA + (cq * 18 + 16) * 512, xoff + (g >> 1) * 512 + (g & 1) * 8);
  gload4(res8b, ws + OFF_XA + (cq * 18 + 16) * 512 + 4, xoff + (g >> 1) * 512 + (g & 1) * 8);
  asm volatile("s_waitcnt lgkmcnt(0)\n\ts_barrier" ::: "memory");
  LAS f32x4* mg = (LAS f32x4*)((unsigned)(uintptr_t)smem + l16);
  {
    const f32x16 t = kh ? o[0] : o[1];
    mg[w * 192] = f32x4{t[0], t[1], t[2], t[3]}; mg[w * 192 + 64] = f32x4{t[4], t[5], t[6], t[7]};
    mg[w * 192 + 128] = f32x4{t[8], t[9], t[10], kh ? m[0] : m[1]};
  }
  asm volatile("s_waitcnt lgkmcnt(0)\n\ts_barrier" ::: "memory");
  const __amdgpu_buffer_rsrc_t ro = __builtin_amdgcn_make_buffer_rsrc((void*)(ws + OFF_OT + (size_t)qb * 16 * 18432), 0, 16 * 18432, 0x00020000);
  {
    const f32x4 in[3] = {mg[(w ^ 4) * 192], mg[(w ^ 4) * 192 + 64], mg[(w ^ 4) * 192 + 128]};
    const float m0 = kh ? m[1] : m[0], m1 = in[2][3], mm = fmaxf(m0, m1);
    const float a0 = __builtin_amdgcn_exp2f(m0 - mm), a1 = __builtin_amdgcn_exp2f(m1 - mm);
    float r_[11];
#pragma unroll
    for (int r = 0; r < 11; ++r) r_[r] = (kh ? o[1][r] : o[0][r]) * a0 + in[r >> 2][r & 3] * a1;
    const float inv = __builtin_amdgcn_rcpf(r_[10]);
    const unsigned ob = ((lane >> 4) & 1) * 18432 + (lane & 15) * 16;
    __builtin_amdgcn_raw_buffer_store_b128(
        not_empty(u32x4{pk2(r_[0] * inv, r_[1] * inv), pk2(r_[2] * inv, r_[3] * inv), pk2(r_[4] * inv, r_[5] * inv), pk2(r_[6] * inv, r_[7] * inv)}), ro,
        ob + hh * 256, (qg * 2 + kh) * 36864 + head * 512, 16);
    if (hh == 0) __builtin_amdgcn_raw_buffer_store_b32(min(pk2(r_[8] * inv, r_[9] * inv), 0xfffffffeu), ro, ob, (qg * 2 + kh) * 36864 + (64 + (head >> 2)) * 256 + (head & 3) * 4, 16);
  }
  STAMP(9);
  LAS f32x4* red = (LAS f32x4*)((unsigned)(uintptr_t)smem + 24576 + l16);
  if (w >= 2) {
    u32x4 af[3][2];
    for (;;) {
      asm volatile("" ::: "memory");
      unsigned mx = 0;
#pragma unroll
      for (int i = 0; i < 6; ++i) af[i >> 1][i & 1] = __builtin_amdgcn_raw_buffer_load_b128(ro, l16 + (i >> 1) * 1024, ((head >> 2) * 2 + (i & 1)) * 18432 + (w - 2) * 3072, 16);
#pragma unroll
      for (int i = 0; i < 6; ++i) mx = max(mx, max4(af[i >> 1][i & 1]));
      if (__all(mx != 0xffffffffu)) break;
      __builtin_amdgcn_s_sleep(ASLEEP);
    }
    STAMP(10);
    f32x4 pa[18];
    sfor<0, 58>([&](auto ic) {
      constexpr int i = decltype(ic)::v, k = i % 18 / 6, u = i / 18 * 6 + i % 6;
      if constexpr (i < 54) { pa[u] = MFMA16(wf[k * 9 + u / 2], __builtin_bit_cast(bf16x8, af[k][u & 1]), k ? pa[u] : (f32x4){}); PIN(pa[u]); }
      if constexpr (i >= 16 && (i - 16) % 18 < 6) red[((w - 2) * 18 + (i - 16) / 18 * 6 + (i - 16) % 18) * 64] = pa[(i - 16) / 18 * 6 + (i - 16) % 18];
      SBAR();
    });
  }
  wait_vm_lgkm_bar<0>();
  STAMP(11);
  PIN(res); PIN(res8a); PIN(res8b);
  {
    f32x4 a = {}, b = {};
#pragma unroll
    for (int k = 0; k < 6; ++k) { a += red[(k * 18 + (w >> 1) * 4 + (w & 1)) * 64]; b += red[(k * 18 + (w >> 1) * 4 + 2 + (w & 1)) * 64]; }
    const u32x4 rv = __builtin_bit_cast(u32x4, res);
    const u32x4 ov = {pk2(a[0] + bflo(rv[0]), b[0] + bfhi(rv[0])), pk2(a[1] + bflo(rv[1]), b[1] + bfhi(rv[1])),
                      pk2(a[2] + bflo(rv[2]), b[2] + bfhi(rv[2])), pk2(a[3] + bflo(rv[3]), b[3] + bfhi(rv[3]))};
    st16(ws + OFF_XB + (cq * 18 + (w >> 1) * 4 + g) * 512 + xoff, ov);
    f32x2 st = stats8(ov);
    if (w < 2) {
      a = red[(16 + w) * 64];
#pragma unroll
      for (int k = 1; k < 6; ++k) a += red[(k * 18 + 16 + w) * 64];
      const u32x2 o8 = {pk2(a[0] + bflo(res8a), a[1] + bfhi(res8a)), pk2(a[2] + bflo(res8b), a[3] + bfhi(res8b))};
      st8(ws + OFF_XB + (cq * 18 + 16 + (g >> 1)) * 512 + (g & 1) * 8 + xoff, o8);
      st += stats8(u32x4{o8[0], o8[1], 0u, 0u});
    }
    st[0] += __shfl_xor(st[0], 16); st[1] += __shfl_xor(st[1], 16);
    st[0] += xhalf(st[0]); st[1] += xhalf(st[1]);
    if (lane < 16) st8(ws + OFF_STB + t32 * 4096 + (cq * 2 + (w >> 2)) * 512 + ((w & 1) * 16 + lane) * 16 + ((w >> 1) & 1) * 8, st);
  }
  PROF_END(ws, FIRST ? 5 : 1);
}

constexpr int UP_SLOT = 36 * 1024, UP_CS = 4 * UP_SLOT + 9 * 128, UP_LDS = UP_CS + 11 * 1024;
constexpr int UP_D = 4;
constexpr int up_x0(int j) { return j >= 15 ? 36 : (12 * j + 4) / 5; }
constexpr int up_n(int j) { return 1 + up_x0(j + 1) - up_x0(j); }
constexpr int up_c(int r) { return r < 112 ? r & 3 : r < 120 ? 0 : r < 144 ? 1 + (r - 120) % 3 : r / 36; }
constexpr int up_s(int r) { return r < 112 ? r >> 2 : r < 120 ? r - 84 : r < 144 ? 28 + (r - 120) / 3 : r % 36; }
#define UPX 5
#define UPO 1
constexpr int UP_X = UPX;
constexpr int up_after(int j) { if (j > 16) return j < 19 ? 4 : 0; int n = 0; for (int i = j + 1; i < j + UP_D; ++i) n += up_n(i) + (i >= UP_X && i < UP_X + 3 ? 2 : 0); return n; }
__global__ __launch_bounds__(512, 2) void k_up(const char* __restrict__ xt, char* __restrict__ ws) {
  extern __shared__ __attribute__((aligned(16))) char smem[];
  const int tid = threadIdx.x, lane = tid & 63, hh = lane >> 5, w = __builtin_amdgcn_readfirstlane(tid >> 6);
  const int xcd = blockIdx.x & 7, idx = blockIdx.x >> 3;
  const int mt = (xcd & 1) * 4 + (idx & 3), nt = (xcd >> 1) * 8 + (idx >> 2);
  const int tt = mt * 8 + w;
  PROF_DECL;
  const unsigned lds0 = (unsigned)(uintptr_t)smem;
  const char* wsrc = ws + OFF_W13 + (size_t)nt * 9 * UP_SLOT;
  const char* xsrc = xt + (size_t)tt * 36 * 1024;
  const unsigned l16 = lane * 16;
  float* csl = (float*)(smem + 4 * UP_SLOT);
  const int sw4 = w * 4096, sw1 = 32768 + (w & 3) * 1024;
  auto stage = [&](auto cc, auto ic) {
    constexpr int c = decltype(cc)::v, i = decltype(ic)::v;
    if constexpr (i == 0) glds16<0>(wsrc + c * UP_SLOT + sw4, l16, lds0 + (c & 3) * UP_SLOT + w * 4096);
    else if constexpr (i < 4) glds<i * 1024>(wsrc + c * UP_SLOT + sw4, l16);
    else glds16<0>(wsrc + c * UP_SLOT + sw1, l16, lds0 + (c & 3) * UP_SLOT + 32768 + (w & 3) * 1024);
  };
  bf16x8 xf[36], stt[4];
  auto item = [&](auto jc, auto ic) {
    constexpr int j = decltype(jc)::v, i = decltype(ic)::v;
    if constexpr (i >= 4) {
      if constexpr ((j == UP_X || j == UP_X + 1) && (i & 1)) gload16<((j - UP_X) * 2 + i / 6) * 1024>(stt[(j - UP_X) * 2 + i / 6], ws + OFF_STB + tt * 4096, l16);
      if constexpr (j == UP_X + 2 && (i & 1)) { const int o = min(w + (i / 6) * 8, 10) * 1024; glds16<0>(ws + OFF_CS13 + nt * 10368 + o, l16, lds0 + UP_CS + o); }
    } else if constexpr (i < up_n(j)) {
      if constexpr (i == 0) {
        int off;
        if constexpr (j < 14) off = (w & 3) * UP_SLOT + (w >> 2) * 1024 + j * 2048;
        else if constexpr (j == 14) off = (28 + w) * 1024;
        else if constexpr (j < 18) { const int q = 8 * (j - 15) + opaque(w), s = (q * 43) >> 7; off = (1 + q - 3 * s) * UP_SLOT + (28 + s) * 1024; }
        else off = (8 * (j - 18) + (j < 22 ? w : w & 3)) * 1024;
        glds16<0>(wsrc + off + (j < 18 ? 0 : 4 * UP_SLOT), l16, lds0 + off);
      } else {
        constexpr int s = up_x0(j) + i - 1;
        gload16<(s & 3) * 1024>(xf[s], xsrc + (s >> 2) * 4096, l16);
      }
    }
  };
  sfor<0, UP_D>([&](auto jc) { sfor<0, 4>([&](auto ic) { item(jc, ic); }); });
  unsigned lb[4];
#pragma unroll
  for (int c = 0; c < 4; ++c) { lb[c] = lds0 + l16 + c * UP_SLOT; PIN(lb[c]); }
  unsigned csb = lds0 + 4 * UP_SLOT + hh * 64; PIN(csb);
  float nmu, ke, ve;
  f32x16 acc[9];
  f32x4 cs[37];
  u32x4 hv;
  const __amdgpu_buffer_rsrc_t rh =
      __builtin_amdgcn_make_buffer_rsrc((void*)(ws + OFF_HT + (size_t)(tt * 2) * 144 * 1024 + (size_t)nt * 9 * 512), 0, 2 * 144 * 1024, 0x00020000);
  const unsigned hoff = ((lane >> 4) & 1) * 144 * 1024 + hh * 256 + (lane & 15) * 16;
  auto ldcs = [&](int e) { cs[e] = *(const LAS f32x4*)(csb + (e >> 2) * 128 + (e & 3) * 16); };
  float g0, g1, u0, u1, t0, t1, h0, h1;
  auto epi = [&](auto ec, auto pc) {
    constexpr int e = decltype(ec)::v, c = e >> 2, i = (e & 3) * 2, p = decltype(pc)::v;
    if constexpr (p == 0) {
      const float a0 = acc[c][i], a1 = acc[c][i + 1], a2 = acc[c][8 + i], c0 = cs[e][0], c1 = cs[e][1], c2 = cs[e][2];
      asm volatile("v_fma_f32 %0, %3, %4, %5\n\tv_fma_f32 %1, %3, %6, %7\n\tv_fma_f32 %2, %3, %8, %9"
                   : "=&v"(g0), "=&v"(g1), "=v"(u0) : "v"(nmu), "v"(c0), "v"(a0), "v"(c1), "v"(a1), "v"(c2), "v"(a2));
    } else if constexpr (p == 1) {
      const float a3 = acc[c][9 + i], c3 = cs[e][3];
      asm volatile("v_mul_f32 %0, %3, %4\n\tv_mul_f32 %1, %3, %5\n\tv_fma_f32 %2, %6, %7, %8"
                   : "=&v"(t0), "=&v"(t1), "=v"(u1) : "v"(ke), "v"(g0), "v"(g1), "v"(nmu), "v"(c3), "v"(a3));
    } else if constexpr (p == 2) {
      asm volatile("v_exp_f32 %0, %0\n\tv_exp_f32 %1, %1\n\tv_mul_f32 %2, %2, %3" : "+v"(t0), "+v"(t1), "+v"(g0) : "v"(u0));
      if constexpr (e + 1 < 36) ldcs(e + 1);
    } else if constexpr (p == 3) {
      asm volatile("v_fma_f32 %0, %0, %3, %3\n\tv_fma_f32 %1, %1, %3, %3\n\tv_mul_f32 %2, %2, %4" : "+v"(t0), "+v"(t1), "+v"(g1) : "v"(ve), "v"(u1));
    } else if constexpr (p == 4) {
      asm volatile("v_rcp_f32 %0, %0\n\tv_rcp_f32 %1, %1" : "+v"(t0), "+v"(t1));
    } else if constexpr (p == 5) {
      asm volatile("v_mul_f32 %0, %2, %3\n\tv_mul_f32 %1, %4, %5" : "=&v"(h0), "=v"(h1) : "v"(g0), "v"(t0), "v"(g1), "v"(t1));
    } else {
      unsigned d;
      asm volatile("v_cvt_pk_bf16_f32 %0, %1, %2" : "=v"(d) : "v"(h0), "v"(h1));
      hv[e & 3] = d;
      if constexpr ((e & 3) == 3) __builtin_amdgcn_raw_buffer_store_b128(hv, rh, hoff + c * 512, 0, HAUX);
    }
  };
  constexpr int PD = 4;
  bf16x8 ring[324];
  sfor<0, 324 + PD>([&](auto jc) {
    constexpr int r = decltype(jc)::v, m = r - PD;
    if constexpr (r < 160) {
      constexpr int j = r >> 3;
      if constexpr ((r & 7) == 0) {
        wait_vm_bar<up_after(j)>();
#pragma unroll
        for (int s = up_x0(j); s < up_x0(j + 1); ++s) PIN(xf[s]);
        if constexpr (j == 0) STAMP(1);
        if constexpr (j == UP_X + 2) {
          const int csi = tid < 288 ? tid : 287;
          float cst = 0.f;
#pragma unroll
          for (int i = 0; i < 9; ++i) cst += csl[288 + i * 288 + csi];
          csl[(csi & ~15) + (csi & 6) * 2 + ((csi >> 3) & 1) * 2 + (csi & 1)] = cst;
          PIN(stt[0]); PIN(stt[1]); PIN(stt[2]); PIN(stt[3]);
          float mu, rstd;
          stats_fin(__builtin_bit_cast(f32x4, stt[0]) + __builtin_bit_cast(f32x4, stt[2]), __builtin_bit_cast(f32x4, stt[1]) + __builtin_bit_cast(f32x4, stt[3]), mu, rstd, ve);
          nmu = -mu; ke = rstd * -1.4426950408889634f;
          PIN(nmu); PIN(ke); PIN(ve);
        }
      }
      if constexpr (j < 17) item(IC<j + UP_D>{}, IC<(r & 1) == UPO ? (r & 7) / 2 : 4 + (r & 7) / 2>{});
      if constexpr (r == 132 || r == 136) item(IC<r == 132 ? 21 : 22>{}, IC<0>{});
      if constexpr (r == 144) STAMP(2);
    }
    if constexpr (r < 324) {
      constexpr int c = up_c(r), s = up_s(r);
      if constexpr (c > 4 && s == 0) { wait_vm_bar<c < 8 ? 5 : 2>(); STAMP(c - 2); }
      if constexpr (c == 4 && s >= 8 && s < 33 && ((s - 8) % 5 == 0 || (s - 8) % 5 == 2)) {
        constexpr int i = (s - 8) / 5 * 2 + (s - 8) % 5 / 2;
        stage(IC<5 + i / 5>{}, IC<i % 5>{});
      }
      if constexpr ((c == 5 || c == 6) && s % 6 == 2 && s < 30) stage(IC<c + 2>{}, IC<s / 6>{});
      ring[r] = ldsr(lb[c & 3] + s * 1024);
    }
    if constexpr (m >= 0) {
      constexpr int c = up_c(m), s = up_s(m), q = m - 122;
      if constexpr (s == 0) acc[c] = MFMA32(ring[m], xf[s], (f32x16){}); else acc[c] = MFMA32(ring[m], xf[s], acc[c]);
      PIN(acc[c]);
      if constexpr (q == -4) ldcs(0);
      if constexpr (q >= 0) {
        SBAR();
        if constexpr (q < 192) epi(IC<q / 6>{}, IC<q % 6>{});
        if constexpr (q >= 6 && q % 6 == 4) epi(IC<q / 6 - 1>{}, IC<6>{});
      }
    }
    SBAR();
  });
  STAMP(8);
  {
    ldcs(33); ldcs(34); ldcs(35);
    float h[8];
#pragma unroll
    for (int i = 0; i < 8; ++i) {
      const float g = acc[8][i] + cs[32 + (i >> 1)][i & 1] * nmu, u = acc[8][8 + i] + cs[32 + (i >> 1)][2 + (i & 1)] * nmu;
      h[i] = (g * u) * __builtin_amdgcn_rcpf(fmaf(__builtin_amdgcn_exp2f(g * ke), ve, ve));
    }
    __builtin_amdgcn_raw_buffer_store_b128(u32x4{pk2(h[0], h[1]), pk2(h[2], h[3]), pk2(h[4], h[5]), pk2(h[6], h[7])}, rh, hoff, 8 * 512, HAUX);
  }
  PROF_END(ws, 3);
}

constexpr int DN_EX = 96 * 1024, DN_LDS = DN_EX + 56 * 1024, DN_D = 4;
constexpr int dn_s(int j) { return j > 15 ? 18 : (8 * j + 2) / 7; }
constexpr int dn_n(int j) { return j > 20 ? 0 : 2 + 2 * (dn_s(j + 1) - dn_s(j)); }
constexpr int dn_after(int j) { int n = j == 18 ? 1 : j > 18 ? 7 : 0; for (int i = j + 1; i < j + DN_D; ++i) n += dn_n(i); return n; }
template <bool FINAL>
__global__ __launch_bounds__(512, 2) void k_down(char* __restrict__ ws, char* __restrict__ out) {
  extern __shared__ __attribute__((aligned(16))) char smem[];
  const int tid = threadIdx.x, lane = tid & 63, w = __builtin_amdgcn_readfirstlane(tid >> 6);
  const int xcd = blockIdx.x & 7, idx = blockIdx.x >> 3;
  const int kq = xcd >> 1, mt = (xcd & 1) * 8 + (idx & 7), cq = idx >> 3, tg = w & 3, kh = w >> 2;
  PROF_DECL;
  const unsigned lds0 = (unsigned)(uintptr_t)smem, l16 = lane * 16;
  const char* wsrc = ws + OFF_W2 + (size_t)(cq * 8 + kq * 2 + kh) * 162 * 1024;
  const char* hsrc = ws + OFF_HT + (size_t)((mt * 8 + tg * 2 + kh) * 144 + kq * 36 + kh * 18) * 1024;
  const int hstep = kh ? -144 * 1024 : 144 * 1024;
  bf16x8 hf[18][2];
  auto item = [&](auto jc, auto ic) {
    constexpr int j = decltype(jc)::v, i = decltype(ic)::v;
    if constexpr (i < dn_n(j)) {
      const int p = j < 20 ? 2 * tg : 0;
      if constexpr (i == 0) glds16<0>(wsrc + (8 * j + p) * 1024, l16, lds0 + kh * 49152 + (8 * j % 48 + p) * 1024);
      else if constexpr (i == 1) glds<1024>(wsrc + (8 * j + p) * 1024, l16);
      else { constexpr int s = dn_s(j) + (i - 2) / 2; gload16<(s & 3) * 1024>(hf[s][i & 1], hsrc + (i & 1) * hstep + (s >> 2) * 4096, l16); }
    }
  };
  sfor<0, DN_D>([&](auto jc) { sfor<0, 6>([&](auto ic) { item(jc, ic); }); });
  const int tok = (mt * 8 + tg * 2 + kh) * 16 + (lane & 15), g = lane >> 4;
  const unsigned xoff = (tok >> 5) * 72 * 512 + (tok & 31) * 16;
  bf16x8 res; unsigned res8a, res8b;
  const __amdgpu_buffer_rsrc_t rx = __builtin_amdgcn_make_buffer_rsrc((void*)(ws + OFF_EX + (size_t)(mt * 4 + cq) * 16 * 10240), 0, 16 * 10240, 0x00020000);
  unsigned lb = lds0 + kh * 49152 + l16; PIN(lb);
  LAS f32x4* exw = (LAS f32x4*)(lds0 + DN_EX + w * 7168 + l16);
  const LAS f32x4* exr = (const LAS f32x4*)(lds0 + DN_EX + (w ^ 4) * 7168 + l16);
  f32x4 acc[9][2], pin[7];
  constexpr int PD = 4;
  bf16x8 ring[162];
  sfor<0, 162 + PD>([&](auto jc) {
    constexpr int r = decltype(jc)::v, m = r - PD;
    if constexpr (r < 162) {
      constexpr int j = r >> 3;
      if constexpr ((r & 7) == 0) {
        if constexpr (j == 17) asm volatile("s_waitcnt lgkmcnt(4)" ::: "memory");
        wait_vm_bar<dn_after(j)>();
#pragma unroll
        for (int s = dn_s(j); s < dn_s(j + 1); ++s) { PIN(hf[s][0]); PIN(hf[s][1]); }
        if constexpr (j == 0) STAMP(1);
        if constexpr (j == 10) STAMP(2);
        if constexpr (j == 17) STAMP(7);
      }
      if constexpr (dn_n(j + DN_D) <= 4) { if constexpr (r & 1) item(IC<j + DN_D>{}, IC<(r & 7) / 2>{}); }
      else if constexpr ((r & 3) != 2) item(IC<j + DN_D>{}, IC<(r & 7) - ((r & 7) + 1) / 4>{});
      if constexpr (r >= 125 && r < 132) exw[(r - 125) * 64] = acc[r - 125][1];
      if constexpr (r >= 136 && r < 143) pin[r - 136] = exr[(r - 136) * 64];
      ring[r] = ldsr(lb + (r % 48) * 1024);
      if constexpr (r == 141 || r == 143 || r == 145) { acc[r - 141][0] += pin[r - 141]; acc[r - 140][0] += pin[r - 140]; PIN(acc[r - 141][0]); PIN(acc[r - 140][0]); }
      if constexpr (r == 142 || r == 144 || r == 146) {
        const f32x4 a = acc[r - 142][0], b = acc[r - 141][0];
        __builtin_amdgcn_raw_buffer_store_b128(not_empty(u32x4{pk2(a[0], b[0]), pk2(a[1], b[1]), pk2(a[2], b[2]), pk2(a[3], b[3])}), rx, l16,
                                               ((((kq + 1 + (r - 142) / 2) & 3) * 4 + 2 - (r - 142) / 2) * 10 + w) * 1024, 16);
      }
      if constexpr (r == 147) {
        acc[6][0] += pin[6];
        __builtin_amdgcn_raw_buffer_store_b128(not_empty(__builtin_bit_cast(u32x4, acc[6][0])), rx, l16, ((tg * 4 + ((kq - tg - 1) & 3)) * 10 + 8 + kh) * 1024, 16);
      }
      if constexpr (r == 148) gload16<0>(res, ws + OFF_XB + (cq * 18 + kq * 4) * 512, xoff + g * 512);
      if constexpr (r == 149) gload4(res8a, ws + OFF_XB + (cq * 18 + 16) * 512, xoff + (g >> 1) * 512 + (g & 1) * 8);
      if constexpr (r == 150) gload4(res8b, ws + OFF_XB + (cq * 18 + 16) * 512 + 4, xoff + (g >> 1) * 512 + (g & 1) * 8);
    }
    if constexpr (m >= 0) {
      constexpr int s = m < 126 ? m / 7 : (m - 126) / 2, c = m < 126 ? m % 7 : 7 + (m & 1);
#pragma unroll
      for (int t = 0; t < 2; ++t) {
        if constexpr (s == 0) acc[c][t] = MFMA16(ring[m], hf[s][t], (f32x4){}); else acc[c][t] = MFMA16(ring[m], hf[s][t], acc[c][t]);
        PIN(acc[c][t]);
      }
    }
    SBAR();
  });
  STAMP(3);
  exw[0] = acc[7][1]; exw[64] = acc[8][1];
  asm volatile("s_waitcnt lgkmcnt(0)\n\ts_barrier" ::: "memory");
  f32x4 v[9];
  v[6] = acc[7][0] + exr[0]; v[7] = acc[8][0] + exr[64]; v[8] = acc[6][0];
  STAMP(4);
  u32x4 in[3], in8[3] = {};
  for (;;) {
    asm volatile("" ::: "memory");
#pragma unroll
    for (int d = 0; d < 3; ++d) in[d] = __builtin_amdgcn_raw_buffer_load_b128(rx, l16, ((kq * 4 + d) * 10 + w) * 1024, 16);
    if (tg == kq) {
#pragma unroll
      for (int d = 0; d < 3; ++d) in8[d] = __builtin_amdgcn_raw_buffer_load_b128(rx, l16, ((kq * 4 + d) * 10 + 8 + kh) * 1024, 16);
    }
    const unsigned mx = max(max(max(max4(in[0]), max4(in[1])), max(max4(in[2]), max4(in8[0]))), max(max4(in8[1]), max4(in8[2])));
    if (__all(mx != 0xffffffffu)) break;
    __builtin_amdgcn_s_sleep(DSLEEP);
  }
  STAMP(6);
  wait_vm<0>();
  PIN(res); PIN(res8a); PIN(res8b);
  const u32x4 rv = __builtin_bit_cast(u32x4, res);
  u32x4 ov;
#pragma unroll
  for (int i = 0; i < 4; ++i) {
    float lo = v[6][i], hi = v[7][i];
#pragma unroll
    for (int d = 0; d < 3; ++d) { lo += bflo(in[d][i]); hi += bfhi(in[d][i]); }
    ov[i] = pk2(lo + bflo(rv[i]), hi + bfhi(rv[i]));
  }
  if (FINAL && !FWT) *(u32x4*)(out + (size_t)tok * (D_ * 2) + (cq * 18 + kq * 4 + g) * 16) = ov;
  else if (FINAL) st16(out + (size_t)tok * (D_ * 2) + (cq * 18 + kq * 4 + g) * 16, ov);
  else st16(out + (cq * 18 + kq * 4 + g) * 512 + xoff, ov);
  f32x2 st = stats8(ov);
  if (tg == kq) {
    f32x4 a = v[8];
#pragma unroll
    for (int d = 0; d < 3; ++d) a += __builtin_bit_cast(f32x4, in8[d]);
    const u32x2 o8 = {pk2(a[0] + bflo(res8a), a[1] + bfhi(res8a)), pk2(a[2] + bflo(res8b), a[3] + bfhi(res8b))};
    if (FINAL && !FWT) *(u32x2*)(out + (size_t)tok * (D_ * 2) + (cq * 18 + 16) * 16 + g * 8) = o8;
    else if (FINAL) st8(out + (size_t)tok * (D_ * 2) + (cq * 18 + 16) * 16 + g * 8, o8);
    else st8(out + (cq * 18 + 16 + (g >> 1)) * 512 + (g & 1) * 8 + xoff, o8);
    st += stats8(u32x4{o8[0], o8[1], 0u, 0u});
  }
  if (!FINAL) {
    st[0] += __shfl_xor(st[0], 16); st[1] += __shfl_xor(st[1], 16);
    st[0] += xhalf(st[0]); st[1] += xhalf(st[1]);
    if (lane < 16) st8(ws + OFF_STA + (tok >> 5) * 4096 + (cq * 2 + (kq >> 1)) * 512 + (tok & 31) * 16 + (kq & 1) * 8, st);
  }
  PROF_END(ws, 4);
}

extern "C" void kernel_launch(void* const* d_in, const int* in_sizes, int n_in, void* d_out, int out_size, void* d_ws,
                              size_t ws_size, hipStream_t stream) {
  const char* x = (const char*)d_in[0];
  const char* wqkv = (const char*)d_in[1];
  const char* wo = (const char*)d_in[2];
  const char* w1 = (const char*)d_in[4];
  const char* w3 = (const char*)d_in[5];
  const char* w2 = (const char*)d_in[6];
  char* ws = (char*)d_ws;
  (void)hipFuncSetAttribute((const void*)k_qkv, hipFuncAttributeMaxDynamicSharedMemorySize, QKV_LDS);
  (void)hipFuncSetAttribute((const void*)k_attn<true>, hipFuncAttributeMaxDynamicSharedMemorySize, ATT_LDS);
  (void)hipFuncSetAttribute((const void*)k_attn<false>, hipFuncAttributeMaxDynamicSharedMemorySize, ATT_LDS);
  (void)hipFuncSetAttribute((const void*)k_up, hipFuncAttributeMaxDynamicSharedMemorySize, UP_LDS);
  (void)hipFuncSetAttribute((const void*)k_down<false>, hipFuncAttributeMaxDynamicSharedMemorySize, DN_LDS);
  (void)hipFuncSetAttribute((const void*)k_down<true>, hipFuncAttributeMaxDynamicSharedMemorySize, DN_LDS);
  k_prep<<<NJ_ALL, 64, 0, stream>>>(x, wqkv, wo, ws);
  for (int l = 0; l < 2; ++l) {
    k_qkv<<<256, 256, QKV_LDS, stream>>>(ws + OFF_XA, ws);
    if (l == 0) k_attn<true><<<256, 512, ATT_LDS, stream>>>(ws, w1, w3, w2);
    else k_attn<false><<<256, 512, ATT_LDS, stream>>>(ws, w1, w3, w2);
    k_up<<<256, 512, UP_LDS, stream>>>(ws + OFF_XB, ws);
    if (l == 0) k_down<false><<<256, 512, DN_LDS, stream>>>(ws, ws + OFF_XA);
    else k_down<true><<<256, 512, DN_LDS, stream>>>(ws, (char*)d_out);
  }
}
```

```cpp
#include <hip/hip_runtime.h>
#include <hip/hip_bf16.h>
#include <stdint.h>

typedef short bf16x8 __attribute__((ext_vector_type(8)));
typedef short s16x4 __attribute__((ext_vector_type(4)));
typedef float f32x16 __attribute__((ext_vector_type(16)));
typedef float f32x4 __attribute__((ext_vector_type(4)));
typedef unsigned u32x4 __attribute__((ext_vector_type(4)));
typedef unsigned u32x2 __attribute__((ext_vector_type(2)));
typedef __bf16 bf2_t __attribute__((ext_vector_type(2)));
typedef float f32x2 __attribute__((ext_vector_type(2)));
#define LAS __attribute__((address_space(3)))
#define DI __device__ __forceinline__

constexpr int S_ = 2048, D_ = 576, H_ = 32, DH_ = 18, F_ = 4608;
constexpr int KS16 = D_ / 16;
constexpr size_t SZ_WQ = 64u * 36 * 1024, SZ_WO = 8u * 18 * 4608, SZ_W13 = 288u * 36 * 1024, SZ_W2 = 8u * 144 * 4608;
constexpr size_t SZ_CSQ = 9u * 64 * 32 * 4, SZ_CS13 = 9u * 288 * 32 * 4, SZ_X = (size_t)S_ * D_ * 2;
constexpr int KV_HALF = 77824, KV_HEAD = 2 * KV_HALF, KV_K2 = 32768, KV_V = 36864;
constexpr size_t SZ_KV = (size_t)H_ * KV_HEAD, SZ_QT = (size_t)H_ * 65536, SZ_Q2 = (size_t)H_ * 8192, SZ_HT = (size_t)S_ * F_ * 2;
constexpr size_t OFF_WQ = 0, OFF_WO = OFF_WQ + SZ_WQ, OFF_W13 = OFF_WO + SZ_WO, OFF_W2 = OFF_W13 + SZ_W13,
                 OFF_CSQ = OFF_W2 + SZ_W2, OFF_CS13 = OFF_CSQ + SZ_CSQ, OFF_XA = OFF_CS13 + SZ_CS13, OFF_XB = OFF_XA + SZ_X,
                 OFF_KV = OFF_XB + SZ_X, OFF_QT = OFF_KV + SZ_KV, OFF_Q2 = OFF_QT + SZ_QT, OFF_OT = OFF_Q2 + SZ_Q2,
                 OFF_HT = OFF_OT + SZ_X, OFF_STA = OFF_HT + SZ_HT, OFF_STB = OFF_STA + S_ * 128,
                 OFF_EX = OFF_STB + S_ * 128, WS_TOTAL = OFF_EX + 64u * 4 * 4 * 10240;

#define MFMA32(a, b, c) __builtin_amdgcn_mfma_f32_32x32x16_bf16(a, b, c, 0, 0, 0)
#define MFMA16(a, b, c) __builtin_amdgcn_mfma_f32_16x16x32_bf16(a, b, c, 0, 0, 0)
#define SBAR() __builtin_amdgcn_sched_barrier(0)
#define PIN(x) asm volatile("" : "+v"(x))

#define PROF_DECL
#define STAMP(i)
#define PROF_END(wsp, kid)
DI unsigned pk2(float lo, float hi) { f32x2 v = {lo, hi}; return __builtin_bit_cast(unsigned, __builtin_convertvector(v, bf2_t)); }
DI float bflo(unsigned u) { return __uint_as_float(u << 16); }
DI float bfhi(unsigned u) { return __uint_as_float(u & 0xffff0000u); }
DI float xhalf(float v) {
  auto rr = __builtin_amdgcn_permlane32_swap(__float_as_uint(v), __float_as_uint(v), false, false);
  return (threadIdx.x & 32) ? __uint_as_float(rr[0]) : __uint_as_float(rr[1]);
}
DI void set_m0(unsigned lds_base) { asm volatile("s_mov_b32 m0, %0" ::"s"(lds_base) : "memory"); }
template <int OFF> DI void glds(const char* base, unsigned voff) {
  asm volatile("global_load_lds_dwordx4 %0, %1 offset:%2" ::"v"(voff), "s"(base), "i"(OFF) : "memory");
}
template <int OFF> DI void glds16(const char* base, unsigned voff, unsigned lds_base) {
  asm volatile("s_mov_b32 m0, %2\n\ts_nop 0\n\tglobal_load_lds_dwordx4 %0, %1 offset:%3" ::"v"(voff), "s"(base), "s"(lds_base), "i"(OFF) : "memory");
}
template <int OFF> DI void gload16(bf16x8& d, const char* base, unsigned voff) {
  asm volatile("global_load_dwordx4 %0, %1, %2 offset:%3" : "=v"(d) : "v"(voff), "s"(base), "i"(OFF) : "memory");
}
template <int OFF> DI void bload16(bf16x8& d, const __amdgpu_buffer_rsrc_t& rs, unsigned voff, int soff) {
  asm volatile("buffer_load_dwordx4 %0, %1, %2, %3 offen offset:%4" : "=v"(d) : "v"(voff), "s"(rs), "s"(soff), "i"(OFF) : "memory");
}
DI void gload4(unsigned& d, const char* base, unsigned voff) {
  asm volatile("global_load_dword %0, %1, %2" : "=v"(d) : "v"(voff), "s"(base) : "memory");
}
#define WT 1
DI void st16(char* p, const u32x4& v) { if (WT) asm volatile("global_store_dwordx4 %0, %1, off sc1\n\ts_nop 1" ::"v"(p), "v"(v) : "memory"); else *(u32x4*)p = v; }
DI void st8(char* p, const u32x2& v) { asm volatile("global_store_dwordx2 %0, %1, off sc1" ::"v"(p), "v"(v) : "memory"); }
DI void st8(char* p, const f32x2& v) { st8(p, __builtin_bit_cast(u32x2, v)); }
DI void st4(char* p, float v) { asm volatile("global_store_dword %0, %1, off sc1" ::"v"(p), "v"(v) : "memory"); }
#define PWT 1
DI void PST(char* base, unsigned off, const u32x4& v) {
  if (PWT) asm volatile("global_store_dwordx4 %0, %1, %2 sc1\n\ts_nop 1" ::"v"(off), "v"(v), "s"(base) : "memory"); else *(u32x4*)(base + off) = v;
}
template <int N> DI void wait_vm() { asm volatile("s_waitcnt vmcnt(%0)" ::"i"(N) : "memory"); }
template <int N> DI void wait_vm_bar() { asm volatile("s_waitcnt vmcnt(%0)\n\ts_barrier" ::"i"(N) : "memory"); }
template <int N> DI void wait_vm_lgkm_bar() { asm volatile("s_waitcnt vmcnt(%0) lgkmcnt(0)\n\ts_barrier" ::"i"(N) : "memory"); }
template <int I> struct IC { static constexpr int v = I; };
template <int B, int E, class Fn> DI void sfor(Fn&& f) { if constexpr (B < E) { f(IC<B>{}); sfor<B + 1, E>(f); } }
DI bf16x8 ldsr(unsigned addr) { return *(const LAS bf16x8*)addr; }
#define HAUX 16
#define DSLEEP 2
#define WIT 4
#define ASLEEP 4
#define PHI (T == 0)
#define PLO (T == 3)
#define ROT 0
DI int rot9(int v) { return v >= 9 ? v - 9 : v; }
DI int opaque(int v) { asm volatile("" : "+s"(v)); return v; }

#define TOUCH(d, sym, line, bytes) \
  asm volatile("s_getpc_b64 vcc\n\ts_add_u32 vcc_lo, vcc_lo, " sym "@rel32@lo+4\n\ts_addc_u32 vcc_hi, vcc_hi, " sym "@rel32@hi+12\n\ts_load_dword %0, vcc, %1" \
               : "+s"(d) : "s"(min((line) * 128, (bytes) - 4)) : "scc", "vcc")
#define KEEP(d) asm volatile("s_waitcnt lgkmcnt(0)" ::"s"(d))
DI void touch_qkv(unsigned& d, int line) { TOUCH(d, "_Z5k_qkvPKcPc", line, 6600); }
DI void touch_attn(unsigned& d, int line) { TOUCH(d, "_Z6k_attnILb1EEvPcPKcS2_S2_", line, 41368); }
DI void touch_up(unsigned& d, int line) { TOUCH(d, "_Z4k_upPKcPc", line, 13484); }
DI void touch_down(unsigned& d, int line) { TOUCH(d, "_Z6k_downPcS_", line, 8724); }
DI u32x4 not_empty(u32x4 v) {
#pragma unroll
  for (int i = 0; i < 4; ++i) v[i] = min(v[i], 0xfffffffeu);
  return v;
}
DI unsigned max4(const u32x4& v) { return max(max(v[0], v[1]), max(v[2], v[3])); }
template <int OFF> DI void mark_empty(const char* base, unsigned voff) {
  asm volatile("global_store_dwordx4 %0, %1, %2 offset:%3 sc1\n\ts_nop 1" ::"v"(voff), "v"(u32x4{~0u, ~0u, ~0u, ~0u}), "s"(base), "i"(OFF) : "memory");
}
DI f32x2 stats8(const u32x4& v) {
  float s1 = 0.f, s2 = 0.f;
#pragma unroll
  for (int i = 0; i < 4; ++i) { const float lo = bflo(v[i]), hi = bfhi(v[i]); s1 += lo + hi; s2 = fmaf(lo, lo, fmaf(hi, hi, s2)); }
  return f32x2{s1, s2};
}
constexpr int NJW = 648 * 3;
struct WJob { int kind, kb, nb; };
DI WJob wjob(int job) {
  WJob j;
  if (job < 1296) { j.kind = job >= 648; const int q = job - j.kind * 648; j.kb = q / 72; j.nb = q % 72; }
  else { j.kind = 2; const int q = job - 1296; j.kb = q / 9; j.nb = q % 9; }
  return j;
}
struct WLoad { __amdgpu_buffer_rsrc_t rs; unsigned voff; int soff, st[5]; };
DI WLoad wjob_src(const WJob& j, int lane, const char* src) {
  const int ld = j.kind < 2 ? F_ * 2 : j.kind == 4 ? 3 * D_ * 2 : D_ * 2;
  int r0 = j.kb * 64, rk = 8, ra = 16, rb = 32, j0 = 1, j1 = 2, j2 = 4;
  if (j.kind == 2) { ra = 1; j0 = 2; j1 = 4; j2 = 16; }
  if (j.kind == 3) {
    if (j.kb < 8) { r0 = j.kb * 72; rk = 1; ra = 18; rb = 36; j0 = 2; j1 = 4; j2 = 8; }
    else { r0 = 16; rk = 72; ra = 144; rb = 288; j1 = 18; j2 = 36; }
  }
  return WLoad{__builtin_amdgcn_make_buffer_rsrc((void*)(src + (size_t)r0 * ld + j.nb * 128), 0, 1 << 20, 0x00020000),
               (unsigned)((lane >> 5) * rk * ld + (lane & 31) * 4), 0, {j0 * ld, j1 * ld, j2 * ld, ra * ld, rb * ld}};
}
template <int K> DI void wjob_load(WLoad& l, unsigned (&r)[32]) {
  constexpr int i = K ^ (K >> 1), b = K ? __builtin_ctz(K | 32) : 0;
  if constexpr (K > 0) {
    if constexpr ((i >> b) & 1) asm volatile("s_add_i32 %0, %0, %1" : "+s"(l.soff) : "s"(l.st[b]) : "scc");
    else asm volatile("s_sub_i32 %0, %0, %1" : "+s"(l.soff) : "s"(l.st[b]) : "scc");
  }
  asm volatile("buffer_load_dword %0, %1, %2, %3 offen" : "=v"(r[i]) : "v"(l.voff), "s"(l.rs), "s"(l.soff) : "memory");
}
struct WStore { unsigned off, es, p1, p2, cso; f32x16 ca[2]; };
DI WStore wjob_dst(const WJob& j, int lane) {
  const int kg = lane >> 5, cp = lane & 31, n = j.nb * 64 + cp * 2;
  unsigned off, es, p1 = 1024, p2 = 2048, cso = 0, ce = 0;
  if (j.kind < 2) {
    const int chunk = j.nb * 4 + (cp >> 4) * 2, rho = cp & 15;
    off = OFF_W13 + ((chunk * 72 + j.kb * 8 + kg) * 32 + j.kind * 16 + rho) * 16; es = 36864;
    const int ck = chunk + kg;
    cso = OFF_CS13 + ((ck + ck / 9 * 72 + j.kb * 9) * 32 + ((rho >> 2) & 1) * 16 + j.kind * 8 + (rho >> 3) * 4 + (rho & 3)) * 4;
  } else if (j.kind == 2) {
    const int cq = n / 144, cc = n % 144, s = (j.kb % 9) * 2, pr = ((cc >> 5) - j.kb / 18 - 1) & 3;
    off = OFF_W2 + (cq * 8 + j.kb / 9) * 162 * 1024 + kg * 256; p1 = 512; p2 = 7168;
    if (cc >= 128) { off += (s * 7 + 6) * 1024 + (cc - 128) * 16; es = 16; }
    else {
      off += (cc & 31) * 8; es = 1024;
      if (pr < 3) off += (s * 7 + pr * 2) * 1024; else { off += (126 + s * 2) * 1024; p2 = 2048; }
    }
  } else if (j.kind == 3) {
    const int cq = n / 144, cc = n % 144;
    off = OFF_WO + (cq * 18 + j.kb * 2) * 9216 + kg * 256; p1 = 512; p2 = 9216;
    if (cc < 128) { off += (cc >> 5) * 2048 + (cc & 31) * 8; es = 1024; }
    else { off += 8192 + (cc - 128) * 16; es = 16; }
  } else {
    const int part = n / D_, col = n % D_, h = col / DH_, d = col % DH_;
    int chunk, rho;
    if (d < 16) { chunk = part == 2 ? 2 * h + 1 : 2 * h; rho = (part == 0 ? 16 : 0) + (d >> 1); es = 128; ce = 16; }
    else { chunk = 2 * h + 1; rho = part == 0 ? 24 : 20 - 2 * part; es = 16; ce = 4; }
    off = OFF_WQ + ((chunk * 72 + j.kb * 8 + kg) * 32 + rho) * 16;
    cso = OFF_CSQ + ((j.kb * 64 + chunk) * 32 + ((rho >> 2) & 1) * 16 + (rho >> 3) * 4 + (rho & 3)) * 4;
  }
  WStore d;
  d.off = off; d.es = es; d.p1 = p1; d.p2 = p2; d.cso = cso + kg * ce;
  return d;
}
template <int K> DI void wjob_store(WStore& d, int lane, const unsigned (&r)[32], char* ws) {
  if constexpr (K < 8) {
    constexpr int g = K ^ (K >> 1), e = g & 1, pp = g >> 1;
    u32x4 v;
#pragma unroll
    for (int q = 0; q < 4; ++q) v[q] = __builtin_amdgcn_perm(r[pp * 8 + 2 * q + 1], r[pp * 8 + 2 * q], e ? 0x07060302u : 0x05040100u);
    if constexpr (K > 0) d.off += K == 2 ? d.p1 : K == 4 ? d.p2 : K == 6 ? -d.p1 : e ? d.es : -d.es;
    PST(ws, d.off, v);
    const u32x4 one = {0x3f803f80u, 0x3f803f80u, 0x3f803f80u, 0x3f803f80u};
    d.ca[e] = MFMA32(__builtin_bit_cast(bf16x8, one), __builtin_bit_cast(bf16x8, v), K > 1 ? d.ca[e] : (f32x16){});
    PIN(d.ca[e]);
  } else {
    st4(ws + d.cso, lane >= 32 ? d.ca[1][0] : d.ca[0][0]);
  }
}

constexpr int NJ_Q = 36 * 9, NJ_ALL = NJ_Q + 256;
__global__ __launch_bounds__(256) void k_prep(const char* __restrict__ x, const char* __restrict__ wqkv, const char* __restrict__ wo,
                                              char* __restrict__ ws) {
  __shared__ float red[256];
  const int lane = threadIdx.x & 63, w = __builtin_amdgcn_readfirstlane(threadIdx.x >> 6), job = blockIdx.x;
  unsigned td = 0;
  touch_qkv(td, (job >> 3) * 4 + w);
  if (job >= 256) {
    const int nb = (job - 256) % 36;
    const WJob j = {nb < 27 ? 4 : 3, (job - 256) / 36, nb < 27 ? nb : nb - 27};
    unsigned r[32];
    WLoad l = wjob_src(j, lane, nb < 27 ? wqkv : wo);
    l.soff = (w & 1) * l.st[3] + (w >> 1) * l.st[4];
    sfor<0, 8>([&](auto k) { wjob_load<decltype(k)::v>(l, r); });
    WStore d = wjob_dst(j, lane);
    d.off += (w & 1) * d.p1 + (w >> 1) * d.p2;
    wait_vm<0>();
#pragma unroll
    for (int i = 0; i < 8; ++i) PIN(r[i]);
    wjob_store<0>(d, lane, r, ws); wjob_store<1>(d, lane, r, ws);
    red[threadIdx.x] = lane >= 32 ? d.ca[1][0] : d.ca[0][0];
    asm volatile("s_waitcnt lgkmcnt(0)\n\ts_barrier" ::: "memory");
    if (w == 0 && nb < 27) st4(ws + d.cso, (red[lane] + red[64 + lane]) + (red[128 + lane] + red[192 + lane]));
  } else {
    const int tok = job * 8 + (lane & 7), pg = lane >> 3;
    const char* src = x + (size_t)tok * (D_ * 2) + (w * 8 + pg) * 16;
    char* dst = ws + OFF_XA + ((size_t)((tok >> 5) * 72 + w * 8 + pg) * 32 + (tok & 31)) * 16;
    u32x4 v[3] = {*(const u32x4*)src, *(const u32x4*)(src + 512), {}};
    if (w == 0) v[2] = *(const u32x4*)(src + 1024);
    PIN(v[0]); PIN(v[1]); PIN(v[2]);
    st16(dst, v[0]); st16(dst + 16384, v[1]);
    if (w == 0) st16(dst + 32768, v[2]);
    f32x2 a = stats8(v[0]) + stats8(v[1]) + stats8(v[2]);
    a[0] += __shfl_xor(a[0], 8); a[1] += __shfl_xor(a[1], 8);
    a[0] += __shfl_xor(a[0], 16); a[1] += __shfl_xor(a[1], 16);
    if ((pg ^ job) & 1) a = f32x2{};
    if (!(pg & 2)) st8(ws + OFF_STA + (tok >> 5) * 4096 + ((job & 1) * 8 + w * 2 + (pg >> 2)) * 256 + ((tok & 16) + (lane & 15)) * 8, a);
  }
  KEEP(td);
}

DI unsigned stats_off(int lane) { return ((lane & 1) + 2 * (lane >> 5)) * 256 + ((lane & 31) >> 1) * 16; }
DI float xone(float v) { return __builtin_bit_cast(float, __builtin_amdgcn_update_dpp(0, __builtin_bit_cast(int, v), 0xB1, 0xf, 0xf, true)); }
DI void stats_fin(const f32x4& a, const f32x4& b, float& mu, float& rstd, float& ve) {
  const f32x4 v = a + b;
  const bool odd = threadIdx.x & 1;
  float s1 = (odd ? v[2] : v[0]) + xone(odd ? v[0] : v[2]), s2 = (odd ? v[3] : v[1]) + xone(odd ? v[1] : v[3]);
  s1 += xhalf(s1); s2 += xhalf(s2);
  mu = s1 * (1.f / D_);
  const float var = fmaxf(s2 * (1.f / D_) - mu * mu, 0.f);
  ve = var + 1e-5f;
  rstd = __builtin_amdgcn_rsqf(ve);
}

constexpr int QKV_ST = 4 * 36 * 1024 + 512, QKV_LDS = QKV_ST + 8 * 1536;
__global__ __launch_bounds__(256, 2) void k_qkv(const char* __restrict__ xt, char* __restrict__ ws) {
  extern __shared__ __attribute__((aligned(16))) char smem[];
  const int tid = threadIdx.x, lane = tid & 63, hh = lane >> 5, w = __builtin_amdgcn_readfirstlane(tid >> 6);
  const int xcd = blockIdx.x & 7, idx = blockIdx.x >> 3;
  const int mt = (xcd & 3) * 4 + (idx & 3), hp = (xcd >> 2) * 8 + (idx >> 2);
  const int tt = mt * 4 + w;
  PROF_DECL;
  const unsigned lds0 = (unsigned)(uintptr_t)smem;
  const char* wsrc = ws + OFF_WQ + (size_t)(hp * 4 + w) * 36 * 1024;
  const char* xsrc = xt + (size_t)tt * 36 * 1024;
  const unsigned l16 = lane * 16;
  float* csl = (float*)(smem + 4 * 36 * 1024);
  bf16x8 xf[36];
  constexpr int QD = 4;
  const int rot = ROT ? rot9((idx & 3) * 2 + (idx >> 2)) : 0;
  auto issue = [&](auto ic) {
    constexpr int s = decltype(ic)::v >> 1;
    if constexpr (s < 36) {
      const int o = rot9((s >> 2) + rot) * 4096;
      if constexpr (decltype(ic)::v & 1) glds<(s & 3) * 1024>(wsrc + o, l16);
      else {
        if constexpr ((s & 3) == 0) set_m0(lds0 + w * 36864 + (s >> 2) * 4096);
        gload16<(s & 3) * 1024>(xf[s], xsrc + o, l16);
      }
    }
  };
  sfor<0, 6 * QD>(issue);
  unsigned csr[9];
#pragma unroll
  for (int i = 0; i < 9; ++i) gload4(csr[i], ws + OFF_CSQ + (hp * 128 + i * 2048) * 4, (tid & 127) * 4);
  bf16x8 sta, stb, stc, std_;
  gload16<0>(sta, ws + OFF_STA + tt * 4096, stats_off(lane));
  gload16<1024>(stb, ws + OFF_STA + tt * 4096, stats_off(lane));
  gload16<2048>(stc, ws + OFF_STA + tt * 4096, stats_off(lane));
  gload16<3072>(std_, ws + OFF_STA + tt * 4096, stats_off(lane));
  unsigned td = 0;
  touch_attn(td, idx * 4 + w); touch_attn(td, idx * 4 + w + 128); touch_attn(td, idx * 4 + w + 256);
  f32x16 acc[4] = {};
  unsigned lb[4];
#pragma unroll
  for (int c = 0; c < 4; ++c) { lb[c] = lds0 + l16 + c * 36864; PIN(lb[c]); }
  constexpr float QS = 0.23570226039551584f * 1.4426950408889634f;
  float rstd, rsq, nmr, nmq;
  f32x4 cm[16];
  constexpr int PD = 6;
  bf16x8 ring[144];
  sfor<0, 144 + PD>([&](auto jc) {
    constexpr int r = decltype(jc)::v, m = r - PD;
    if constexpr (r < 144 && r % 12 == 0) {
      constexpr int g = r / 12;
      wait_vm_bar<6 * (g + QD <= 12 ? QD - 1 : 11 - g) + (g < QD ? 13 : 0)>();
      if constexpr (g % 3 == 0) STAMP(1 + g / 3);
      if constexpr (g == QD) {
        float cst = 0.f;
#pragma unroll
        for (int i = 0; i < 9; ++i) { PIN(csr[i]); cst += __uint_as_float(csr[i]); }
        csl[tid & 127] = cst;
      }
#pragma unroll
      for (int s = 3 * g; s < 3 * g + 3; ++s) PIN(xf[s]);
    }
    if constexpr (r < 144 && r % 2 == 0) issue(IC<6 * QD + r / 2>{});
    if constexpr (r == 98) {
      float mu, ve;
      PIN(sta); PIN(stb); PIN(stc); PIN(std_);
      stats_fin(__builtin_bit_cast(f32x4, sta) + __builtin_bit_cast(f32x4, stc), __builtin_bit_cast(f32x4, stb) + __builtin_bit_cast(f32x4, std_), mu, rstd, ve);
      nmr = -mu * rstd; rsq = rstd * QS; nmq = nmr * QS;
    }
    if constexpr (r >= 100 && r < 132 && (r & 1) == 0) cm[(r - 100) / 2] = *(const f32x4*)(csl + (r - 100) / 8 * 32 + hh * 16 + (r - 100) / 2 % 4 * 4);
    if constexpr (r >= 107 && r < 139 && (r & 1)) {
      constexpr int i = (r - 107) / 2;
      const float f = (i & 7) == 2 || (i & 7) == 3 || (i & 7) == 7 ? nmq : nmr;
#pragma unroll
      for (int e = 0; e < 4; ++e) { float t = cm[i][e] * f; PIN(t); cm[i][e] = t; }
    }
    if constexpr (r == 134 || r == 137 || r == 140) mark_empty<(r - 134) / 3 * 1024>(ws + OFF_OT + (size_t)blockIdx.x * 9216 + w * 2048, l16);
    if constexpr (r < 144) ring[r] = ldsr(lb[r & 3] + (r >> 2) * 1024);
    if constexpr (m >= 0) {
      acc[m & 3] = MFMA32(ring[m], xf[m >> 2], acc[m & 3]);
      PIN(acc[m & 3]);
    }
    SBAR();
  });
  STAMP(7);
  const int tok = tt * 32 + (lane & 31);
#pragma unroll
  for (int hq = 0; hq < 2; ++hq) {
    const int head = hp * 2 + hq;
    float v[2][16];
#pragma unroll
    for (int c = 0; c < 2; ++c)
#pragma unroll
      for (int r4 = 0; r4 < 4; ++r4) {
#pragma unroll
        for (int i = 0; i < 4; ++i) v[c][r4 * 4 + i] = fmaf(acc[hq * 2 + c][r4 * 4 + i], r4 > 1 + c ? rsq : rstd, cm[hq * 8 + c * 4 + r4][i]);
      }
    char* kv = ws + OFF_KV + (size_t)head * KV_HEAD + (tok >> 10) * KV_HALF;
    st16(kv + (tt & 31) * 1024 + lane * 16, u32x4{pk2(v[0][0], v[0][1]), pk2(v[0][2], v[0][3]), pk2(v[0][4], v[0][5]), pk2(v[0][6], v[0][7])});
    st16(ws + OFF_QT + (size_t)head * 65536 + tt * 1024 + lane * 16,
         u32x4{pk2(v[0][8], v[0][9]), pk2(v[0][10], v[0][11]), pk2(v[0][12], v[0][13]), pk2(v[0][14], v[0][15])});
    const unsigned sb = lds0 + QKV_ST + (w * 2 + hq) * 1536, vr = sb + (lane & 31) * 40;
    *(LAS u32x2*)(vr + hh * 16) = u32x2{pk2(v[1][0], v[1][1]), pk2(v[1][2], v[1][3])};
    *(LAS u32x2*)(vr + hh * 16 + 8) = u32x2{pk2(v[1][4], v[1][5]), pk2(v[1][6], v[1][7])};
    if (hh == 0) {
      *(LAS u32x2*)(vr + 32) = u32x2{pk2(v[1][8], v[1][9]), 0x00003f80u};
      *(LAS unsigned*)(sb + 1280 + lane * 4) = pk2(v[1][10], v[1][11]);
      *(LAS unsigned*)(sb + 1408 + lane * 4) = pk2(v[1][12], v[1][13]);
    }
  }
  {
    const unsigned sb = lds0 + QKV_ST + w * 3072;
    const unsigned kvo = OFF_KV + hp * 2 * KV_HEAD + (tt >> 5) * KV_HALF, vo = kvo + KV_V + (tt & 31) * 1280;
    asm volatile("s_waitcnt lgkmcnt(0)" ::: "memory");
    const u32x4 a = *(const LAS u32x4*)(sb + l16), b = *(const LAS u32x4*)(sb + 1536 + l16);
    const u32x4 c = *(const LAS u32x4*)(sb + hh * 1536 + 1024 + (lane & 31) * 16);
    PST(ws + vo, l16, a);
    PST(ws + vo + KV_HEAD, l16, b);
    const int i = lane & 31;
    PST(ws, i < 16 ? vo + hh * KV_HEAD + 1024 + i * 16 : i < 24 ? kvo + hh * KV_HEAD + KV_K2 + (tt & 31) * 128 + (i - 16) * 16
                                                                 : (unsigned)OFF_Q2 + (hp * 2 + hh) * 8192 + tt * 128 + (i - 24) * 16, c);
  }
  KEEP(td);
  PROF_END(ws, 0);
}

constexpr int ATT_LDS = KV_HEAD + 2048;
#define MFMA8(a, b, c) __builtin_amdgcn_mfma_f32_32x32x8bf16_1k(a, b, c, 0, 0, 0)
DI s16x4 vtr(unsigned p) { return __builtin_amdgcn_ds_read_tr16_b64_v4i16((LAS s16x4*)p); }
DI bf16x8 vtr2(unsigned p) { return __builtin_shufflevector(vtr(p), vtr(p + 320), 0, 1, 2, 3, 4, 5, 6, 7); }
DI float max16(const f32x16& s) {
  float a = fmaxf(fmaxf(s[0], s[1]), s[2]), b = fmaxf(fmaxf(s[3], s[4]), s[5]);
  a = fmaxf(fmaxf(a, s[6]), s[7]); b = fmaxf(fmaxf(b, s[8]), s[9]);
  a = fmaxf(fmaxf(a, s[10]), s[11]); b = fmaxf(fmaxf(b, s[12]), s[13]);
  a = fmaxf(fmaxf(a, s[14]), s[15]);
  return fmaxf(a, b);
}
DI s16x4 make_qb(unsigned q2, float m, int hh) {
  const float nm = -m;
  const float h0 = bfhi(__float_as_uint(nm)), r0 = nm - h0, h1 = bfhi(__float_as_uint(r0)), r1 = r0 - h1;
  u32x2 u = {q2, (__float_as_uint(h0) >> 16) | __float_as_uint(h1)};
  if (hh) u = u32x2{0u, __float_as_uint(r1) >> 16};
  return __builtin_bit_cast(s16x4, u);
}
DI void pin16(f32x16& v) { PIN(v); }
#define EX(i) e[i] = __builtin_amdgcn_exp2f(sc[i]); PIN(e[i])

template <bool FIRST>
__global__ __launch_bounds__(512, 2) void k_attn(char* __restrict__ ws, const char* __restrict__ w1, const char* __restrict__ w3,
                                                 const char* __restrict__ w2) {
  extern __shared__ __attribute__((aligned(16))) char smem[];
  const int tid = threadIdx.x, lane = tid & 63, hh = lane >> 5, w = __builtin_amdgcn_readfirstlane(tid >> 6);
  const int xcd = blockIdx.x & 7, idx = blockIdx.x >> 3;
  const int head = xcd * 4 + (idx & 3), qb = idx >> 2;
  const int qg = w & 3, kh = w >> 2;
  PROF_DECL;
  const unsigned lds0 = (unsigned)(uintptr_t)smem + kh * KV_HALF, l16 = lane * 16;
  const char* kvsrc = ws + OFF_KV + (size_t)head * KV_HEAD + kh * KV_HALF;
  bf16x8 qa[2]; unsigned q2[2];
#pragma unroll
  for (int q = 0; q < 2; ++q) {
    const int qt = qb * 8 + qg * 2 + (q ^ kh);
    gload16<0>(qa[q], ws + OFF_QT + (size_t)head * 65536 + qt * 1024, l16);
    gload4(q2[q], ws + OFF_Q2 + (size_t)head * 8192 + qt * 128, (lane & 31) * 4);
  }
#pragma unroll
  for (int i = 0; i < 5; ++i) {
    int p = qg * 5 + i; p = p > 18 ? 18 : p;
    const int off = (p < 8 ? p : p == 8 ? 32 : 27 + p) * 1024;
    glds16<0>(kvsrc + off, l16, lds0 + off);
  }
  auto late = [&](int i) {
    const int q = opaque(qg);
    const int off = (i < 6 ? 8 + 4 * i + q : i == 6 ? (q == 3 ? 46 : 33 + q) : i < 14 ? 19 + 4 * i + q : 75) * 1024;
    glds16<0>(kvsrc + off, l16, lds0 + off);
  };
  __builtin_amdgcn_s_setprio(1);
  const int i16 = lane & 15;
  const unsigned kp0 = lds0 + l16, k2p0 = lds0 + KV_K2 + (lane & 31) * 4;
  const unsigned vp0 = lds0 + KV_V + (4 * hh + (i16 >> 2)) * 40 + ((lane & 16) ? 32 : (i16 & 3) * 8);
  const f32x16 zero = {};
  f32x16 o[2];
  float m[2] = {0.f, 0.f};
  u32x2 kb = {0u, 0x3f803f80u};
  PIN(kb);
  STAMP(1);
  wait_vm_bar<0>();
  STAMP(2);
  PIN(qa[0]); PIN(qa[1]); PIN(q2[0]); PIN(q2[1]);
  bf16x8 wf[27];
  const __amdgpu_buffer_rsrc_t rwo = __builtin_amdgcn_make_buffer_rsrc((void*)(ws + OFF_WO + ((head & 3) * 18 + w * 3 - 6) * 9216), 0, w < 2 ? 0 : 27648, 0x00020000);
  auto wo_load = [&](auto ic) {
    constexpr int i = decltype(ic)::v;
    if constexpr (i < 27) bload16<(i & 3) * 1024>(wf[i], rwo, l16, (i >> 2) * 4096);
  };
  WLoad wl;
  WStore wd;
  unsigned wr[32];
  for (int pass = 0; pass < 2; ++pass) {
    const s16x4 qb2[2] = {make_qb(q2[0], m[0], hh), make_qb(q2[1], m[1], hh)};
    o[0] = zero; o[1] = zero;
    unsigned kp = kp0, k2p = k2p0, vp = vp0;
    PIN(kp); PIN(k2p); PIN(vp);
    bf16x8 ka = ldsr(kp);
    kb[0] = *(const LAS unsigned*)k2p;
    bf16x8 v0 = vtr2(vp), v1 = vtr2(vp + 640);
    f32x16 sa = MFMA32(ka, qa[0], zero), sb;
    sa = MFMA8(__builtin_bit_cast(s16x4, kb), qb2[0], sa);
    u32x4 p1 = {0u, 0u, 0u, 0u};
    float ep[4] = {0.f, 0.f, 0.f, 0.f};
    auto half = [&](auto qc, auto tc, const f32x16& sc, f32x16& sn) {
      constexpr int Q = decltype(qc)::v, T = decltype(tc)::v;
      float e[16]; u32x4 p0;
      sn = MFMA32(ka, qa[Q ^ 1], zero);
      SBAR();
      if constexpr (Q == 0) ka = ldsr(kp + (T + 1) * 1024);
      p1[2] = pk2(ep[0], ep[1]); p1[3] = pk2(ep[2], ep[3]); PIN(p1); EX(0); EX(1); EX(2); EX(3);
      SBAR();
      o[Q ^ 1] = MFMA32(v1, __builtin_bit_cast(bf16x8, p1), o[Q ^ 1]);
      SBAR();
      if constexpr (Q == 0) v1 = vtr2(vp + T * 1280 + 640);
      p0[0] = pk2(e[0], e[1]); p0[1] = pk2(e[2], e[3]); SBAR(); EX(4); EX(5); EX(6); EX(7);
      SBAR();
      sn = MFMA8(__builtin_bit_cast(s16x4, kb), qb2[Q ^ 1], sn);
      SBAR();
      if constexpr (Q == 0) kb[0] = *(const LAS unsigned*)(k2p + (T + 1) * 128);
      p0[2] = pk2(e[4], e[5]); p0[3] = pk2(e[6], e[7]); PIN(p0); EX(8); EX(9); EX(10); EX(11);
      SBAR();
      o[Q] = MFMA32(v0, __builtin_bit_cast(bf16x8, p0), o[Q]);
      SBAR();
      if constexpr (Q == 1) v0 = vtr2(vp + (T + 1) * 1280);
      p1[0] = pk2(e[8], e[9]); p1[1] = pk2(e[10], e[11]); SBAR(); EX(12); EX(13); EX(14); EX(15);
      ep[0] = e[12]; ep[1] = e[13]; ep[2] = e[14]; ep[3] = e[15];
      SBAR();
    };
    auto iter = [&](auto mc) {
      constexpr int IT = decltype(mc)::v;
      sfor<0, 4>([&](auto tc) {
        constexpr int T = decltype(tc)::v;
        if constexpr (PHI) asm volatile("s_cmp_eq_u32 %0, 0\n\ts_cbranch_scc1 1f\n\ts_setprio 2\n1:" ::"s"(kh) : "scc");
        if constexpr (PLO) asm volatile("s_cmp_eq_u32 %0, 0\n\ts_cbranch_scc1 1f\n\ts_setprio 0\n1:" ::"s"(kh) : "scc");
        if constexpr (IT == 0) { late(4 * T); late(4 * T + 1); }
        if constexpr (IT == 1 && T == 3) { STAMP(3); wait_vm_bar<FIRST ? 6 : 0>(); STAMP(4); }
        if constexpr (FIRST && IT >= 3 && IT < 7 && T == 0) {
          wait_vm<IT == 3 ? 12 : IT < 6 ? 10 : 2>();
#pragma unroll
          for (int i = 0; i < 8; ++i) PIN(wr[((IT - 3) ^ ((IT - 3) >> 1)) * 8 + i]);
        }
        if constexpr (FIRST && IT >= 1 && IT < 5) wjob_load<IT * 8 - 8 + T * 2>(wl, wr);
        if constexpr (IT == 6 || IT == 7) { wo_load(IC<(IT - 6) * 16 + T * 4>{}); wo_load(IC<(IT - 6) * 16 + T * 4 + 1>{}); }
        SBAR();
        half(IC<0>{}, tc, sa, sb);
        if constexpr (IT == 0) { late(4 * T + 2); if (T < 3) late(4 * T + 3); }
        if constexpr (FIRST && IT >= 1 && IT < 5) wjob_load<IT * 8 - 7 + T * 2>(wl, wr);
        if constexpr (FIRST && IT >= 3 && IT < 7 && (T & 1) == 0) wjob_store<(IT - 3) * 2 + T / 2>(wd, lane, wr, ws);
        if constexpr (IT == 2)
          mark_empty<T * 1024>(ws + OFF_EX + (size_t)blockIdx.x * 40960 + min(w * 4, 26) * 1024, l16);
        if constexpr (IT == 6 || IT == 7) { wo_load(IC<(IT - 6) * 16 + T * 4 + 2>{}); wo_load(IC<(IT - 6) * 16 + T * 4 + 3>{}); }
        SBAR();
        half(IC<1>{}, tc, sb, sa);
      });
      kp += 4096; k2p += 512; vp += 5120;
      pin16(o[0]); pin16(o[1]);
    };
    if (__builtin_expect(pass == 0, 1)) {
      iter(IC<0>{}); STAMP(6);
      const int job = blockIdx.x * 8 + opaque(w);
      const WJob wj = wjob(job < NJW ? job : job - NJW);
      if (FIRST) wl = wjob_src(wj, lane, wj.kind == 0 ? w1 : wj.kind == 1 ? w3 : w2);
      iter(IC<1>{}); STAMP(7); iter(IC<2>{});
      if (FIRST) {
        int ln = lane; PIN(ln);
        wd = wjob_dst(wj, ln);
        STAMP(8);
        iter(IC<3>{}); iter(IC<4>{}); iter(IC<5>{});
      } else {
        for (int it = 3; it < 6; ++it) iter(IC<9>{});
      }
      iter(IC<6>{});
      if (FIRST && wj.kind < 2) wjob_store<8>(wd, lane, wr, ws);
      iter(IC<7>{});
    } else {
      for (int it = 0; it < 8; ++it) iter(IC<9>{});
      sfor<0, 27>(wo_load);
    }
    wait_vm<0>();
#pragma unroll
    for (int i = 0; i < 27; ++i) PIN(wf[i]);
    p1[2] = pk2(ep[0], ep[1]); p1[3] = pk2(ep[2], ep[3]);
    o[1] = MFMA32(v1, __builtin_bit_cast(bf16x8, p1), o[1]);
    bool ok = true;
#pragma unroll
    for (int q = 0; q < 2; ++q) {
      float a = fabsf(o[q][0]);
#pragma unroll
      for (int r = 1; r < 10; ++r) a += fabsf(o[q][r]);
      ok = ok && a < 0x1p100f && o[q][10] < 0x1p60f && o[q][10] > 0x1p-60f;
    }
    if (__builtin_expect(pass == 1 || __all(ok), 1)) break;
    const s16x4 qz0 = make_qb(q2[0], 0.f, hh), qz1 = make_qb(q2[1], 0.f, hh);
    float mx0 = -__builtin_inff(), mx1 = mx0;
    for (int t = 0; t < 32; ++t) {
      const bf16x8 kk = ldsr(kp0 + t * 1024);
      kb[0] = *(const LAS unsigned*)(k2p0 + t * 128);
      f32x16 s = MFMA32(kk, qa[0], zero); s = MFMA8(__builtin_bit_cast(s16x4, kb), qz0, s); mx0 = fmaxf(mx0, max16(s));
      s = MFMA32(kk, qa[1], zero); s = MFMA8(__builtin_bit_cast(s16x4, kb), qz1, s); mx1 = fmaxf(mx1, max16(s));
    }
    m[0] = fmaxf(mx0, xhalf(mx0)); m[1] = fmaxf(mx1, xhalf(mx1));
  }
  STAMP(5);
  __builtin_amdgcn_s_setprio(0);
  asm volatile("s_waitcnt lgkmcnt(0)\n\ts_barrier" ::: "memory");
  LAS f32x4* mg = (LAS f32x4*)((unsigned)(uintptr_t)smem + l16);
  mg[w * 192] = f32x4{o[1][0], o[1][1], o[1][2], o[1][3]}; mg[w * 192 + 64] = f32x4{o[1][4], o[1][5], o[1][6], o[1][7]};
  mg[w * 192 + 128] = f32x4{o[1][8], o[1][9], o[1][10], m[1]};
  asm volatile("s_waitcnt lgkmcnt(0)\n\ts_barrier" ::: "memory");
  const __amdgpu_buffer_rsrc_t ro = __builtin_amdgcn_make_buffer_rsrc((void*)(ws + OFF_OT + (size_t)qb * 16 * 18432), 0, 16 * 18432, 0x00020000);
  {
    const f32x4 in[3] = {mg[(w ^ 4) * 192], mg[(w ^ 4) * 192 + 64], mg[(w ^ 4) * 192 + 128]};
    const float mm = fmaxf(m[0], in[2][3]);
    float a0 = __builtin_amdgcn_exp2f(m[0] - mm), a1 = __builtin_amdgcn_exp2f(in[2][3] - mm);
    const float inv = __builtin_amdgcn_rcpf(fmaf(o[0][10], a0, in[2][2] * a1));
    a0 *= inv; a1 *= inv;
    float r_[10];
#pragma unroll
    for (int r = 0; r < 10; ++r) r_[r] = fmaf(o[0][r], a0, in[r >> 2][r & 3] * a1);
    const unsigned ob = ((lane >> 4) & 1) * 18432 + (lane & 15) * 16;
    __builtin_amdgcn_raw_buffer_store_b128(
        not_empty(u32x4{pk2(r_[0], r_[1]), pk2(r_[2], r_[3]), pk2(r_[4], r_[5]), pk2(r_[6], r_[7])}), ro,
        ob + hh * 256, (qg * 2 + kh) * 36864 + head * 512, 16);
    if (hh == 0) __builtin_amdgcn_raw_buffer_store_b32(min(pk2(r_[8], r_[9]), 0xfffffffeu), ro, lane * 4, (qg * 2 + kh) * 36864 + (head >> 4) * 18432 + 16384 + (head & 15) * 128, 16);
  }
  STAMP(9);
  SBAR();
  const int cq = head & 3, t32 = qb * 8 + (head >> 2), g = lane >> 4;
  const unsigned xoff = t32 * 72 * 512 + ((w & 1) * 16 + (lane & 15)) * 16;
  bf16x8 res; unsigned res8a, res8b;
  gload16<0>(res, ws + OFF_XA + (cq * 18 + (w >> 1) * 4) * 512, xoff + g * 512);
  gload4(res8a, ws + OFF_XA + (cq * 18 + 16) * 512, xoff + (g >> 1) * 512 + (g & 1) * 8);
  gload4(res8b, ws + OFF_XA + (cq * 18 + 16) * 512 + 4, xoff + (g >> 1) * 512 + (g & 1) * 8);
  unsigned td = 0;
  touch_up(td, idx * 8 + w);
  LAS f32x4* red = (LAS f32x4*)((unsigned)(uintptr_t)smem + 24576 + l16);
  if (w >= 2) {
    u32x4 af[3][2];
    for (;;) {
      asm volatile("" ::: "memory");
      unsigned mx = 0;
#pragma unroll
      for (int i = 0; i < 6; ++i)
        if (i < 2 || w < 7) af[i >> 1][i & 1] = __builtin_amdgcn_raw_buffer_load_b128(ro, l16 + (i >> 1) * 1024, ((head >> 2) * 2 + (i & 1)) * 18432 + (w - 2) * 3072, 16);
      if (w == 7) {
#pragma unroll
        for (int i = 0; i < 16; ++i)
          af[1 + (i >> 3)][(i >> 2) & 1][i & 3] = __builtin_amdgcn_raw_buffer_load_b32(ro, g * 512 + (lane & 15) * 4 + (i & 3) * 128 + ((i >> 2) & 1) * 64,
                                                                                       ((head >> 2) * 2 + (i >> 3)) * 18432 + 16384, 16);
      }
#pragma unroll
      for (int i = 0; i < 6; ++i) mx = max(mx, max4(af[i >> 1][i & 1]));
      if (__all(mx != 0xffffffffu)) break;
      __builtin_amdgcn_s_sleep(ASLEEP);
    }
    STAMP(10);
    f32x4 pa[18];
    sfor<0, 58>([&](auto ic) {
      constexpr int i = decltype(ic)::v, k = i % 18 / 6, u = i / 18 * 6 + i % 6;
      if constexpr (i < 54) { pa[u] = MFMA16(wf[k * 9 + u / 2], __builtin_bit_cast(bf16x8, af[k][u & 1]), k ? pa[u] : (f32x4){}); PIN(pa[u]); }
      if constexpr (i >= 16 && (i - 16) % 18 < 6) red[((w - 2) * 18 + (i - 16) / 18 * 6 + (i - 16) % 18) * 64] = pa[(i - 16) / 18 * 6 + (i - 16) % 18];
      SBAR();
    });
  }
  wait_vm_lgkm_bar<0>();
  STAMP(11);
  PIN(res); PIN(res8a); PIN(res8b);
  {
    f32x4 a = {}, b = {};
#pragma unroll
    for (int k = 0; k < 6; ++k) { a += red[(k * 18 + (w >> 1) * 4 + (w & 1)) * 64]; b += red[(k * 18 + (w >> 1) * 4 + 2 + (w & 1)) * 64]; }
    const u32x4 rv = __builtin_bit_cast(u32x4, res);
    const u32x4 ov = {pk2(a[0] + bflo(rv[0]), b[0] + bfhi(rv[0])), pk2(a[1] + bflo(rv[1]), b[1] + bfhi(rv[1])),
                      pk2(a[2] + bflo(rv[2]), b[2] + bfhi(rv[2])), pk2(a[3] + bflo(rv[3]), b[3] + bfhi(rv[3]))};
    st16(ws + OFF_XB + (cq * 18 + (w >> 1) * 4 + g) * 512 + xoff, ov);
    f32x2 st = stats8(ov);
    if (w < 2) {
      a = red[(16 + w) * 64];
#pragma unroll
      for (int k = 1; k < 6; ++k) a += red[(k * 18 + 16 + w) * 64];
      const u32x2 o8 = {pk2(a[0] + bflo(res8a), a[1] + bfhi(res8a)), pk2(a[2] + bflo(res8b), a[3] + bfhi(res8b))};
      st8(ws + OFF_XB + (cq * 18 + 16 + (g >> 1)) * 512 + (g & 1) * 8 + xoff, o8);
      st += stats8(u32x4{o8[0], o8[1], 0u, 0u});
    }
    st[0] += __shfl_xor(st[0], 16); st[1] += __shfl_xor(st[1], 16);
    st[0] += xhalf(st[0]); st[1] += xhalf(st[1]);
    if (lane < 16) st8(ws + OFF_STB + t32 * 4096 + (cq * 4 + (w >> 1)) * 256 + ((w & 1) * 16 + lane) * 8, st);
  }
  KEEP(td);
  PROF_END(ws, FIRST ? 5 : 1);
}

constexpr int UP_SLOT = 36 * 1024, UP_CS = 4 * UP_SLOT + 9 * 128, UP_LDS = UP_CS + 11 * 1024;
constexpr int UP_D = 4;
constexpr int up_x0(int j) { return j >= 15 ? 36 : (12 * j + 4) / 5; }
constexpr int up_n(int j) { return 1 + up_x0(j + 1) - up_x0(j); }
constexpr int up_c(int r) { return r < 112 ? r & 3 : r < 120 ? 0 : r < 144 ? 1 + (r - 120) % 3 : r / 36; }
constexpr int up_s(int r) { return r < 112 ? r >> 2 : r < 120 ? r - 84 : r < 144 ? 28 + (r - 120) / 3 : r % 36; }
#define UPX 5
#define UPO 1
constexpr int UP_X = UPX;
constexpr int up_after(int j) { if (j > 16) return j < 19 ? 4 : 0; int n = 0; for (int i = j + 1; i < j + UP_D; ++i) n += up_n(i) + (i >= UP_X && i < UP_X + 3 ? 2 : 0); return n; }
__global__ __launch_bounds__(512, 2) void k_up(const char* __restrict__ xt, char* __restrict__ ws) {
  extern __shared__ __attribute__((aligned(16))) char smem[];
  const int tid = threadIdx.x, lane = tid & 63, hh = lane >> 5, w = __builtin_amdgcn_readfirstlane(tid >> 6);
  const int xcd = blockIdx.x & 7, idx = blockIdx.x >> 3;
  const int mt = (xcd & 1) * 4 + (idx & 3), nt = (xcd >> 1) * 8 + (idx >> 2);
  const int tt = mt * 8 + w;
  PROF_DECL;
  const unsigned lds0 = (unsigned)(uintptr_t)smem;
  const char* wsrc = ws + OFF_W13 + (size_t)nt * 9 * UP_SLOT;
  const char* xsrc = xt + (size_t)tt * 36 * 1024;
  const unsigned l16 = lane * 16;
  float* csl = (float*)(smem + 4 * UP_SLOT);
  const int sw4 = w * 4096, sw1 = 32768 + (w & 3) * 1024;
  auto stage = [&](auto cc, auto ic) {
    constexpr int c = decltype(cc)::v, i = decltype(ic)::v;
    if constexpr (i == 0) glds16<0>(wsrc + c * UP_SLOT + sw4, l16, lds0 + (c & 3) * UP_SLOT + w * 4096);
    else if constexpr (i < 4) glds<i * 1024>(wsrc + c * UP_SLOT + sw4, l16);
    else glds16<0>(wsrc + c * UP_SLOT + sw1, l16, lds0 + (c & 3) * UP_SLOT + 32768 + (w & 3) * 1024);
  };
  bf16x8 xf[36], stt[4];
  auto item = [&](auto jc, auto ic) {
    constexpr int j = decltype(jc)::v, i = decltype(ic)::v;
    if constexpr (i >= 4) {
      if constexpr ((j == UP_X || j == UP_X + 1) && (i & 1)) gload16<((j - UP_X) * 2 + i / 6) * 1024>(stt[(j - UP_X) * 2 + i / 6], ws + OFF_STB + tt * 4096, stats_off(lane));
      if constexpr (j == UP_X + 2 && (i & 1)) { const int o = min(w + (i / 6) * 8, 10) * 1024; glds16<0>(ws + OFF_CS13 + nt * 10368 + o, l16, lds0 + UP_CS + o); }
    } else if constexpr (i < up_n(j)) {
      if constexpr (i == 0) {
        int off;
        if constexpr (j < 14) off = (w & 3) * UP_SLOT + (w >> 2) * 1024 + j * 2048;
        else if constexpr (j == 14) off = (28 + w) * 1024;
        else if constexpr (j < 18) { const int q = 8 * (j - 15) + opaque(w), s = (q * 43) >> 7; off = (1 + q - 3 * s) * UP_SLOT + (28 + s) * 1024; }
        else off = (8 * (j - 18) + (j < 22 ? w : w & 3)) * 1024;
        glds16<0>(wsrc + off + (j < 18 ? 0 : 4 * UP_SLOT), l16, lds0 + off);
      } else {
        constexpr int s = up_x0(j) + i - 1;
        gload16<(s & 3) * 1024>(xf[s], xsrc + (s >> 2) * 4096, l16);
      }
    }
  };
  sfor<0, UP_D>([&](auto jc) { sfor<0, 4>([&](auto ic) { item(jc, ic); }); });
  unsigned lb[4];
#pragma unroll
  for (int c = 0; c < 4; ++c) { lb[c] = lds0 + l16 + c * UP_SLOT; PIN(lb[c]); }
  unsigned csb = lds0 + 4 * UP_SLOT + hh * 64; PIN(csb);
  float nmu, ke, ve;
  f32x16 acc[9];
  f32x4 cs[37];
  u32x4 hv;
  const __amdgpu_buffer_rsrc_t rh =
      __builtin_amdgcn_make_buffer_rsrc((void*)(ws + OFF_HT + (size_t)(tt * 2) * 144 * 1024 + (size_t)nt * 9 * 512), 0, 2 * 144 * 1024, 0x00020000);
  const unsigned hoff = ((lane >> 4) & 1) * 144 * 1024 + hh * 256 + (lane & 15) * 16;
  auto ldcs = [&](int e) { cs[e] = *(const LAS f32x4*)(csb + (e >> 2) * 128 + (e & 3) * 16); };
  float g0, g1, u0, u1, t0, t1, h0, h1;
  auto epi = [&](auto ec, auto pc) {
    constexpr int e = decltype(ec)::v, c = e >> 2, i = (e & 3) * 2, p = decltype(pc)::v;
    if constexpr (p == 0) {
      const float a0 = acc[c][i], a1 = acc[c][i + 1], a2 = acc[c][8 + i], c0 = cs[e][0], c1 = cs[e][1], c2 = cs[e][2];
      asm volatile("v_fma_f32 %0, %3, %4, %5\n\tv_fma_f32 %1, %3, %6, %7\n\tv_fma_f32 %2, %3, %8, %9"
                   : "=&v"(g0), "=&v"(g1), "=v"(u0) : "v"(nmu), "v"(c0), "v"(a0), "v"(c1), "v"(a1), "v"(c2), "v"(a2));
    } else if constexpr (p == 1) {
      const float a3 = acc[c][9 + i], c3 = cs[e][3];
      asm volatile("v_mul_f32 %0, %3, %4\n\tv_mul_f32 %1, %3, %5\n\tv_fma_f32 %2, %6, %7, %8"
                   : "=&v"(t0), "=&v"(t1), "=v"(u1) : "v"(ke), "v"(g0), "v"(g1), "v"(nmu), "v"(c3), "v"(a3));
    } else if constexpr (p == 2) {
      asm volatile("v_exp_f32 %0, %0\n\tv_exp_f32 %1, %1\n\tv_mul_f32 %2, %2, %3" : "+v"(t0), "+v"(t1), "+v"(g0) : "v"(u0));
      if constexpr (e + 1 < 36) ldcs(e + 1);
    } else if constexpr (p == 3) {
      asm volatile("v_fma_f32 %0, %0, %3, %3\n\tv_fma_f32 %1, %1, %3, %3\n\tv_mul_f32 %2, %2, %4" : "+v"(t0), "+v"(t1), "+v"(g1) : "v"(ve), "v"(u1));
    } else if constexpr (p == 4) {
      asm volatile("v_rcp_f32 %0, %0\n\tv_rcp_f32 %1, %1" : "+v"(t0), "+v"(t1));
    } else if constexpr (p == 5) {
      asm volatile("v_mul_f32 %0, %2, %3\n\tv_mul_f32 %1, %4, %5" : "=&v"(h0), "=v"(h1) : "v"(g0), "v"(t0), "v"(g1), "v"(t1));
    } else {
      unsigned d;
      asm volatile("v_cvt_pk_bf16_f32 %0, %1, %2" : "=v"(d) : "v"(h0), "v"(h1));
      hv[e & 3] = d;
      if constexpr ((e & 3) == 3) __builtin_amdgcn_raw_buffer_store_b128(hv, rh, hoff + c * 512, 0, HAUX);
    }
  };
  constexpr int PD = 4;
  bf16x8 ring[324];
  unsigned td = 0;
  sfor<0, 324 + PD>([&](auto jc) {
    constexpr int r = decltype(jc)::v, m = r - PD;
    if constexpr (r < 160) {
      constexpr int j = r >> 3;
      if constexpr ((r & 7) == 0) {
        wait_vm_bar<up_after(j)>();
#pragma unroll
        for (int s = up_x0(j); s < up_x0(j + 1); ++s) PIN(xf[s]);
        if constexpr (j == 0) STAMP(1);
        if constexpr (j == UP_X + 2) {
          const int csi = tid < 288 ? tid : 287;
          float cst = 0.f;
#pragma unroll
          for (int i = 0; i < 9; ++i) cst += csl[288 + i * 288 + csi];
          csl[(csi & ~15) + (csi & 6) * 2 + ((csi >> 3) & 1) * 2 + (csi & 1)] = cst;
          PIN(stt[0]); PIN(stt[1]); PIN(stt[2]); PIN(stt[3]);
          float mu, rstd;
          stats_fin(__builtin_bit_cast(f32x4, stt[0]) + __builtin_bit_cast(f32x4, stt[2]), __builtin_bit_cast(f32x4, stt[1]) + __builtin_bit_cast(f32x4, stt[3]), mu, rstd, ve);
          nmu = -mu; ke = rstd * -1.4426950408889634f;
          PIN(nmu); PIN(ke); PIN(ve);
        }
      }
      if constexpr (j < 17) item(IC<j + UP_D>{}, IC<(r & 1) == UPO ? (r & 7) / 2 : 4 + (r & 7) / 2>{});
      if constexpr (r == 132 || r == 136) item(IC<r == 132 ? 21 : 22>{}, IC<0>{});
      if constexpr (r == 144) STAMP(2);
    }
    if constexpr (r < 324) {
      constexpr int c = up_c(r), s = up_s(r);
      if constexpr (c > 4 && s == 0) { wait_vm_bar<c < 8 ? 5 : 2>(); STAMP(c - 2); }
      if constexpr (c == 4 && s >= 8 && s < 33 && ((s - 8) % 5 == 0 || (s - 8) % 5 == 2)) {
        constexpr int i = (s - 8) / 5 * 2 + (s - 8) % 5 / 2;
        stage(IC<5 + i / 5>{}, IC<i % 5>{});
      }
      if constexpr ((c == 5 || c == 6) && s % 6 == 2 && s < 30) stage(IC<c + 2>{}, IC<s / 6>{});
      if constexpr (r == 317) touch_down(td, idx * 8 + w);
      ring[r] = ldsr(lb[c & 3] + s * 1024);
    }
    if constexpr (m >= 0) {
      constexpr int c = up_c(m), s = up_s(m), q = m - 122;
      if constexpr (s == 0) acc[c] = MFMA32(ring[m], xf[s], (f32x16){}); else acc[c] = MFMA32(ring[m], xf[s], acc[c]);
      PIN(acc[c]);
      if constexpr (q == -4) ldcs(0);
      if constexpr (q >= 0) {
        SBAR();
        if constexpr (q < 192) epi(IC<q / 6>{}, IC<q % 6>{});
        if constexpr (q >= 6 && q % 6 == 4) epi(IC<q / 6 - 1>{}, IC<6>{});
      }
    }
    SBAR();
  });
  STAMP(8);
  {
    ldcs(33); ldcs(34); ldcs(35);
    float h[8];
#pragma unroll
    for (int i = 0; i < 8; ++i) {
      const float g = acc[8][i] + cs[32 + (i >> 1)][i & 1] * nmu, u = acc[8][8 + i] + cs[32 + (i >> 1)][2 + (i & 1)] * nmu;
      h[i] = (g * u) * __builtin_amdgcn_rcpf(fmaf(__builtin_amdgcn_exp2f(g * ke), ve, ve));
    }
    __builtin_amdgcn_raw_buffer_store_b128(u32x4{pk2(h[0], h[1]), pk2(h[2], h[3]), pk2(h[4], h[5]), pk2(h[6], h[7])}, rh, hoff, 8 * 512, HAUX);
  }
  KEEP(td);
  PROF_END(ws, 3);
}

constexpr int DN_EX = 96 * 1024, DN_LDS = DN_EX + 56 * 1024, DN_D = 4;
constexpr int dn_s(int j) { return j > 15 ? 18 : (8 * j + 2) / 7; }
constexpr int dn_n(int j) { return j > 20 ? 0 : 2 + 2 * (dn_s(j + 1) - dn_s(j)); }
constexpr int dn_after(int j) { int n = j == 18 ? 1 : j > 18 ? 7 : 0; for (int i = j + 1; i < j + DN_D; ++i) n += dn_n(i); return n; }
__global__ __launch_bounds__(512, 2) void k_down(char* ws, char* out) {
  const bool FINAL = out != ws + OFF_XA;
  extern __shared__ __attribute__((aligned(16))) char smem[];
  const int tid = threadIdx.x, lane = tid & 63, w = __builtin_amdgcn_readfirstlane(tid >> 6);
  const int xcd = blockIdx.x & 7, idx = blockIdx.x >> 3;
  const int kq = xcd >> 1, mt = (xcd & 1) * 8 + (idx & 7), cq = idx >> 3, tg = w & 3, kh = w >> 2;
  PROF_DECL;
  const unsigned lds0 = (unsigned)(uintptr_t)smem, l16 = lane * 16;
  const char* wsrc = ws + OFF_W2 + (size_t)(cq * 8 + kq * 2 + kh) * 162 * 1024;
  const char* hsrc = ws + OFF_HT + (size_t)((mt * 8 + tg * 2 + kh) * 144 + kq * 36 + kh * 18) * 1024;
  const int hstep = kh ? -144 * 1024 : 144 * 1024;
  bf16x8 hf[18][2];
  auto item = [&](auto jc, auto ic) {
    constexpr int j = decltype(jc)::v, i = decltype(ic)::v;
    if constexpr (i < dn_n(j)) {
      const int p = j < 20 ? 2 * tg : 0;
      if constexpr (i == 0) glds16<0>(wsrc + (8 * j + p) * 1024, l16, lds0 + kh * 49152 + (8 * j % 48 + p) * 1024);
      else if constexpr (i == 1) glds<1024>(wsrc + (8 * j + p) * 1024, l16);
      else { constexpr int s = dn_s(j) + (i - 2) / 2; gload16<(s & 3) * 1024>(hf[s][i & 1], hsrc + (i & 1) * hstep + (s >> 2) * 4096, l16); }
    }
  };
  sfor<0, DN_D>([&](auto jc) { sfor<0, 6>([&](auto ic) { item(jc, ic); }); });
  const int tok = (mt * 8 + tg * 2 + kh) * 16 + (lane & 15), g = lane >> 4;
  const unsigned xoff = (tok >> 5) * 72 * 512 + (tok & 31) * 16;
  bf16x8 res; unsigned res8a, res8b;
  const __amdgpu_buffer_rsrc_t rx = __builtin_amdgcn_make_buffer_rsrc((void*)(ws + OFF_EX + (size_t)(mt * 4 + cq) * 16 * 10240), 0, 16 * 10240, 0x00020000);
  unsigned lb = lds0 + kh * 49152 + l16; PIN(lb);
  LAS f32x4* exw = (LAS f32x4*)(lds0 + DN_EX + w * 7168 + l16);
  const LAS f32x4* exr = (const LAS f32x4*)(lds0 + DN_EX + (w ^ 4) * 7168 + l16);
  f32x4 acc[9][2], pin[7];
  constexpr int PD = 4;
  bf16x8 ring[162];
  sfor<0, 162 + PD>([&](auto jc) {
    constexpr int r = decltype(jc)::v, m = r - PD;
    if constexpr (r < 162) {
      constexpr int j = r >> 3;
      if constexpr ((r & 7) == 0) {
        if constexpr (j == 17) asm volatile("s_waitcnt lgkmcnt(4)" ::: "memory");
        wait_vm_bar<dn_after(j)>();
#pragma unroll
        for (int s = dn_s(j); s < dn_s(j + 1); ++s) { PIN(hf[s][0]); PIN(hf[s][1]); }
        if constexpr (j == 0) STAMP(1);
        if constexpr (j == 10) STAMP(2);
        if constexpr (j == 17) STAMP(7);
      }
      if constexpr (dn_n(j + DN_D) <= 4) { if constexpr (r & 1) item(IC<j + DN_D>{}, IC<(r & 7) / 2>{}); }
      else if constexpr ((r & 3) != 2) item(IC<j + DN_D>{}, IC<(r & 7) - ((r & 7) + 1) / 4>{});
      if constexpr (r >= 125 && r < 132) exw[(r - 125) * 64] = acc[r - 125][1];
      if constexpr (r >= 136 && r < 143) pin[r - 136] = exr[(r - 136) * 64];
      ring[r] = ldsr(lb + (r % 48) * 1024);
      if constexpr (r == 141 || r == 143 || r == 145) { acc[r - 141][0] += pin[r - 141]; acc[r - 140][0] += pin[r - 140]; PIN(acc[r - 141][0]); PIN(acc[r - 140][0]); }
      if constexpr (r == 142 || r == 144 || r == 146) {
        const f32x4 a = acc[r - 142][0], b = acc[r - 141][0];
        __builtin_amdgcn_raw_buffer_store_b128(not_empty(u32x4{pk2(a[0], b[0]), pk2(a[1], b[1]), pk2(a[2], b[2]), pk2(a[3], b[3])}), rx, l16,
                                               ((((kq + 1 + (r - 142) / 2) & 3) * 4 + 2 - (r - 142) / 2) * 10 + w) * 1024, 16);
      }
      if constexpr (r == 147) {
        acc[6][0] += pin[6];
        __builtin_amdgcn_raw_buffer_store_b128(not_empty(__builtin_bit_cast(u32x4, acc[6][0])), rx, l16, ((tg * 4 + ((kq - tg - 1) & 3)) * 10 + 8 + kh) * 1024, 16);
      }
      if constexpr (r == 148) gload16<0>(res, ws + OFF_XB + (cq * 18 + kq * 4) * 512, xoff + g * 512);
      if constexpr (r == 149) gload4(res8a, ws + OFF_XB + (cq * 18 + 16) * 512, xoff + (g >> 1) * 512 + (g & 1) * 8);
      if constexpr (r == 150) gload4(res8b, ws + OFF_XB + (cq * 18 + 16) * 512 + 4, xoff + (g >> 1) * 512 + (g & 1) * 8);
    }
    if constexpr (m >= 0) {
      constexpr int s = m < 126 ? m / 7 : (m - 126) / 2, c = m < 126 ? m % 7 : 7 + (m & 1);
#pragma unroll
      for (int t = 0; t < 2; ++t) {
        if constexpr (s == 0) acc[c][t] = MFMA16(ring[m], hf[s][t], (f32x4){}); else acc[c][t] = MFMA16(ring[m], hf[s][t], acc[c][t]);
        PIN(acc[c][t]);
      }
    }
    SBAR();
  });
  STAMP(3);
  exw[0] = acc[7][1]; exw[64] = acc[8][1];
  asm volatile("s_waitcnt lgkmcnt(0)\n\ts_barrier" ::: "memory");
  f32x4 v[9];
  v[6] = acc[7][0] + exr[0]; v[7] = acc[8][0] + exr[64]; v[8] = acc[6][0];
  STAMP(4);
  unsigned td = 0;
  touch_qkv(td, idx * 8 + w);
  u32x4 in[3], in8[3] = {};
  for (;;) {
    asm volatile("" ::: "memory");
#pragma unroll
    for (int d = 0; d < 3; ++d) in[d] = __builtin_amdgcn_raw_buffer_load_b128(rx, l16, ((kq * 4 + d) * 10 + w) * 1024, 16);
    if (tg == kq) {
#pragma unroll
      for (int d = 0; d < 3; ++d) in8[d] = __builtin_amdgcn_raw_buffer_load_b128(rx, l16, ((kq * 4 + d) * 10 + 8 + kh) * 1024, 16);
    }
    const unsigned mx = max(max(max(max4(in[0]), max4(in[1])), max(max4(in[2]), max4(in8[0]))), max(max4(in8[1]), max4(in8[2])));
    if (__all(mx != 0xffffffffu)) break;
    __builtin_amdgcn_s_sleep(DSLEEP);
  }
  STAMP(6);
  wait_vm<0>();
  PIN(res); PIN(res8a); PIN(res8b);
  const u32x4 rv = __builtin_bit_cast(u32x4, res);
  u32x4 ov;
#pragma unroll
  for (int i = 0; i < 4; ++i) {
    float lo = v[6][i], hi = v[7][i];
#pragma unroll
    for (int d = 0; d < 3; ++d) { lo += bflo(in[d][i]); hi += bfhi(in[d][i]); }
    ov[i] = pk2(lo + bflo(rv[i]), hi + bfhi(rv[i]));
  }
  PST(out, FINAL ? tok * (D_ * 2) + (cq * 18 + kq * 4 + g) * 16 : (cq * 18 + kq * 4 + g) * 512 + xoff, ov);
  f32x2 st = stats8(ov);
  if (tg == kq) {
    f32x4 a = v[8];
#pragma unroll
    for (int d = 0; d < 3; ++d) a += __builtin_bit_cast(f32x4, in8[d]);
    const u32x2 o8 = {pk2(a[0] + bflo(res8a), a[1] + bfhi(res8a)), pk2(a[2] + bflo(res8b), a[3] + bfhi(res8b))};
    st8(out + (FINAL ? tok * (D_ * 2) + (cq * 18 + 16) * 16 + g * 8 : (cq * 18 + 16 + (g >> 1)) * 512 + (g & 1) * 8 + xoff), o8);
    st += stats8(u32x4{o8[0], o8[1], 0u, 0u});
  }
  if (!FINAL) {
    st[0] += __shfl_xor(st[0], 16); st[1] += __shfl_xor(st[1], 16);
    st[0] += xhalf(st[0]); st[1] += xhalf(st[1]);
    if (lane < 16) st8(ws + OFF_STA + (tok >> 5) * 4096 + (cq * 4 + kq) * 256 + (tok & 31) * 8, st);
  }
  KEEP(td);
  PROF_END(ws, 4);
}

extern "C" void kernel_launch(void* const* d_in, const int* in_sizes, int n_in, void* d_out, int out_size, void* d_ws,
                              size_t ws_size, hipStream_t stream) {
  const char* x = (const char*)d_in[0];
  const char* wqkv = (const char*)d_in[1];
  const char* wo = (const char*)d_in[2];
  const char* w1 = (const char*)d_in[4];
  const char* w3 = (const char*)d_in[5];
  const char* w2 = (const char*)d_in[6];
  char* ws = (char*)d_ws;
  (void)hipFuncSetAttribute((const void*)k_qkv, hipFuncAttributeMaxDynamicSharedMemorySize, QKV_LDS);
  (void)hipFuncSetAttribute((const void*)k_attn<true>, hipFuncAttributeMaxDynamicSharedMemorySize, ATT_LDS);
  (void)hipFuncSetAttribute((const void*)k_attn<false>, hipFuncAttributeMaxDynamicSharedMemorySize, ATT_LDS);
  (void)hipFuncSetAttribute((const void*)k_up, hipFuncAttributeMaxDynamicSharedMemorySize, UP_LDS);
  (void)hipFuncSetAttribute((const void*)k_down, hipFuncAttributeMaxDynamicSharedMemorySize, DN_LDS);
  k_prep<<<NJ_ALL, 256, 0, stream>>>(x, wqkv, wo, ws);
  for (int l = 0; l < 2; ++l) {
    k_qkv<<<256, 256, QKV_LDS, stream>>>(ws + OFF_XA, ws);
    if (l == 0) k_attn<true><<<256, 512, ATT_LDS, stream>>>(ws, w1, w3, w2);
    else k_attn<false><<<256, 512, ATT_LDS, stream>>>(ws, w1, w3, w2);
    k_up<<<256, 512, UP_LDS, stream>>>(ws + OFF_XB, ws);
    k_down<<<256, 512, DN_LDS, stream>>>(ws, l == 0 ? ws + OFF_XA : (char*)d_out);
  }
}
```
